```python
import torch

def _softmax(x, axis=-1):
    return torch.softmax(x, dim=axis)
def _gelu(x):
    return torch.nn.functional.gelu(x, approximate="tanh")
def _silu(x):
    return torch.nn.functional.silu(x)
def _sigmoid(x):
    return torch.sigmoid(x)
def _layernorm(x, axis=-1, eps=1e-5):
    m = x.mean(dim=axis, keepdim=True)
    v = x.var(dim=axis, keepdim=True, unbiased=False)
    return (x - m) / torch.sqrt(v + eps)
def _scatter_add(tgt, idx, src, axis=0):
    return tgt.clone().index_add_(axis, idx, src)
def _segment_sum(src, seg, n):
    out = torch.zeros((n,) + src.shape[1:], dtype=src.dtype)
    return out.index_add_(0, seg, src)
def _pad(x, pw):
    flat = tuple(v for pair in reversed(pw) for v in pair)
    return torch.nn.functional.pad(x, flat)

kernel_name = 'parallel_sum__sliding_block__dense_activation_gelu__prompt'

def setup_inputs():

    _w = (lambda a: (lambda t: t.to(torch.bfloat16) if t.is_floating_point() else t)(torch.as_tensor(a)))
    def _A_setup():
        B, S, D = 1, 2048, 448
        return {
            "x": _w((lambda *s: torch.randn(*s, dtype=torch.bfloat16) * 0.05)(B, S, D)),
            "wqkv": _w((lambda *s: torch.randn(*s, dtype=torch.bfloat16) * 0.05)(D, 3 * D)),
            "wo": _w((lambda *s: torch.randn(*s, dtype=torch.bfloat16) * 0.05)(D, D)),
        }
    _A_in = _A_setup()

    def _B_setup():
        B, S, D, F = 1, 2048, 448, 1792
        return {
            "x": _w((lambda *s: torch.randn(*s, dtype=torch.bfloat16) * 0.05)(B, S, D)),
            "w1": _w((lambda *s: torch.randn(*s, dtype=torch.bfloat16) * 0.8)(D, F) * (D ** -0.5)),
            "b1": _w((lambda *s: torch.randn(*s, dtype=torch.bfloat16) * 0.8)(F) * 0.01),
            "w2": _w((lambda *s: torch.randn(*s, dtype=torch.bfloat16) * 0.8)(F, D) * (F ** -0.5)),
            "b2": _w((lambda *s: torch.randn(*s, dtype=torch.bfloat16) * 0.8)(D) * 0.01),
        }
    _B_in = _B_setup()

    return {
        "A_x": _A_in["x"],
        "A_wqkv": _A_in["wqkv"],
        "A_wo": _A_in["wo"],
        "B_x": _B_in["x"],
        "B_w1": _B_in["w1"],
        "B_b1": _B_in["b1"],
        "B_w2": _B_in["w2"],
        "B_b2": _B_in["b2"],
    }

def reference(A_x, A_wqkv, A_wo, B_x, B_w1, B_b1, B_w2, B_b2):
    def _A_ref(x, wqkv, wo):
        B, S, D = x.shape
        H = 16; Dh = D // H; W = 256
        qkv = (lambda x, *d: x.permute(*d))((x @ wqkv).reshape(B, S, 3, H, Dh), 2, 0, 3, 1, 4)
        q, k, v = qkv[0], qkv[1], qkv[2]
        scores = torch.einsum("bhqd,bhkd->bhqk", q, k) * (Dh ** -0.5)
        idx = torch.arange(S)
        mask = (idx[:, None] - idx[None, :] >= 0) & (idx[:, None] - idx[None, :] < W)
        scores = torch.where(mask[None, None], scores, -1e9)
        p = _softmax(scores, axis=-1)
        o = torch.einsum("bhqk,bhkd->bhqd", p, v)
        return (lambda x, *d: x.permute(*d))(o, 0, 2, 1, 3).reshape(B, S, D) @ wo

    def _B_ref(x, w1, b1, w2, b2):
        h = torch.einsum("bsd,df->bsf", x, w1) + b1
        h = _gelu(h)
        return torch.einsum("bsf,fd->bsd", h, w2) + b2

    x = A_x
    xn = _layernorm(x, axis=-1)
    return x + _A_ref(xn, A_wqkv, A_wo) + _B_ref(xn, B_w1, B_b1, B_w2, B_b2)
```

```cpp
#include <hip/hip_runtime.h>
#include <hip/hip_bf16.h>
typedef __hip_bfloat16 bf16;
constexpr int S=2048, D=448, H=16, DH=28, W=256, F=1792;
__device__ inline float b2f(bf16 v){return __bfloat162float(v);}
__device__ inline bf16 f2b(float v){return __float2bfloat16(v);}
__device__ inline float rb(float v){return b2f(f2b(v));}

__global__ void k_ln(const bf16* x, bf16* xn){
  int row=blockIdx.x; int l=threadIdx.x;
  float v[7]; float s=0;
  for(int i=0;i<7;i++){v[i]=b2f(x[row*D+l+64*i]); s+=v[i];}
  for(int o=32;o;o>>=1) s+=__shfl_xor(s,o);
  float m=s/D; float q=0;
  for(int i=0;i<7;i++){float d=v[i]-m; q+=d*d;}
  for(int o=32;o;o>>=1) q+=__shfl_xor(q,o);
  float var=q/D;
  float r=rsqrtf(var+1e-5f);
  for(int i=0;i<7;i++) xn[row*D+l+64*i]=f2b((v[i]-m)*r);
}
__global__ void k_gemm(const bf16* A,int lda,const bf16* B,int ldb,bf16* C,int ldc,int K,const bf16* bias,int mode){
  __shared__ float As[16][17], Bs[16][17];
  int tx=threadIdx.x&15, ty=threadIdx.x>>4;
  int row=blockIdx.y*16+ty, col=blockIdx.x*16+tx;
  float acc=0;
  for(int k0=0;k0<K;k0+=16){
    As[ty][tx]=b2f(A[row*lda+k0+tx]);
    Bs[ty][tx]=b2f(B[(k0+ty)*ldb+col]);
    __syncthreads();
    for(int k=0;k<16;k++) acc+=As[ty][k]*Bs[k][tx];
    __syncthreads();
  }
  if(mode==1){ float h=acc+b2f(bias[col]); float t=tanhf(0.7978845608028654f*(h+0.044715f*h*h*h)); acc=0.5f*h*(1.f+t);}
  C[row*ldc+col]=f2b(acc);
}
__global__ void k_attn(const bf16* qkv, bf16* o){
  int q=blockIdx.x, h=blockIdx.y; int t=threadIdx.x;
  __shared__ float p[256]; __shared__ float red[256];
  int key=q-t; float s=-1e30f;
  if(key>=0){ s=0; for(int d=0;d<DH;d++) s+=b2f(qkv[q*3*D+h*DH+d])*b2f(qkv[key*3*D+D+h*DH+d]); s*=rsqrtf((float)DH);}
  red[t]=s; __syncthreads();
  for(int o2=128;o2;o2>>=1){ if(t<o2) red[t]=fmaxf(red[t],red[t+o2]); __syncthreads(); }
  float m=red[0]; __syncthreads();
  float e=key>=0?__expf(s-m):0.f; p[t]=e; red[t]=e; __syncthreads();
  for(int o2=128;o2;o2>>=1){ if(t<o2) red[t]+=red[t+o2]; __syncthreads(); }
  float l=red[0];
  if(t<DH){ float a=0; for(int j=0;j<256;j++){int kk=q-j; if(kk>=0) a+=p[j]*b2f(qkv[kk*3*D+2*D+h*DH+t]);} o[q*2240+h*DH+t]=f2b(a/l);}
}
__global__ void k_final(const bf16* oh,const bf16* wo,const bf16* w2,const bf16* b2,const bf16* x,bf16* out){
  __shared__ float As[16][17], Bs[16][17];
  int tx=threadIdx.x&15, ty=threadIdx.x>>4;
  int row=blockIdx.y*16+ty, col=blockIdx.x*16+tx;
  float acc=0;
  for(int k0=0;k0<2240;k0+=16){
    As[ty][tx]=b2f(oh[row*2240+k0+tx]);
    int k=k0+ty;
    Bs[ty][tx]= k<448? b2f(wo[k*D+col]) : b2f(w2[(k-448)*D+col]);
    __syncthreads();
    for(int kk=0;kk<16;kk++) acc+=As[ty][kk]*Bs[kk][tx];
    __syncthreads();
  }
  out[row*D+col]=f2b(acc+b2f(b2[col])+b2f(x[row*D+col]));
}

extern "C" void kernel_launch(void* const* d_in, const int* in_sizes, int n_in, void* d_out, int out_size, void* d_ws, size_t ws_size, hipStream_t stream){
  const bf16* x=(const bf16*)d_in[0]; const bf16* wqkv=(const bf16*)d_in[1]; const bf16* wo=(const bf16*)d_in[2];
  const bf16* w1=(const bf16*)d_in[4]; const bf16* b1=(const bf16*)d_in[5]; const bf16* w2=(const bf16*)d_in[6]; const bf16* b2=(const bf16*)d_in[7];
  bf16* xn=(bf16*)d_ws; bf16* qkv=xn+S*D; bf16* oh=qkv+S*3*D;
  hipMemsetAsync((char*)d_ws+(200<<20),0,4096,stream);
  k_ln<<<S,64,0,stream>>>(x,xn);
  k_gemm<<<dim3(3*D/16,S/16),256,0,stream>>>(xn,D,wqkv,3*D,qkv,3*D,D,nullptr,0);
  k_gemm<<<dim3(F/16,S/16),256,0,stream>>>(xn,D,w1,F,oh+448,2240,D,b1,1);
  k_attn<<<dim3(S,H),256,0,stream>>>(qkv,oh);
  k_final<<<dim3(D/16,S/16),256,0,stream>>>(oh,wo,w2,b2,x,(bf16*)d_out);
}
```

```cpp
#include <hip/hip_runtime.h>
#include <hip/hip_bf16.h>
typedef __hip_bfloat16 bf16;
constexpr int S=2048, D=448, H=16, DH=28, W=256, F=1792;
__device__ inline float b2f(bf16 v){return __bfloat162float(v);}
__device__ inline bf16 f2b(float v){return __float2bfloat16(v);}
__device__ inline float rb(float v){return b2f(f2b(v));}

__global__ void k_ln(const bf16* x, bf16* xn){
  int row=blockIdx.x; int l=threadIdx.x;
  float v[7]; float s=0;
  for(int i=0;i<7;i++){v[i]=b2f(x[row*D+l+64*i]); s+=v[i];}
  for(int o=32;o;o>>=1) s+=__shfl_xor(s,o);
  float m=s/D; float q=0;
  for(int i=0;i<7;i++){float d=v[i]-m; q+=d*d;}
  for(int o=32;o;o>>=1) q+=__shfl_xor(q,o);
  float var=q/D;
  float r=rsqrtf(var+1e-5f);
  for(int i=0;i<7;i++) xn[row*D+l+64*i]=f2b((v[i]-m)*r);
}
__global__ void k_gemm(const bf16* A,int lda,const bf16* B,int ldb,bf16* C,int ldc,int K,const bf16* bias,int mode){
  __shared__ float As[16][17], Bs[16][17];
  int tx=threadIdx.x&15, ty=threadIdx.x>>4;
  int row=blockIdx.y*16+ty, col=blockIdx.x*16+tx;
  float acc=0;
  for(int k0=0;k0<K;k0+=16){
    As[ty][tx]=b2f(A[row*lda+k0+tx]);
    Bs[ty][tx]=b2f(B[(k0+ty)*ldb+col]);
    __syncthreads();
    for(int k=0;k<16;k++) acc+=As[ty][k]*Bs[k][tx];
    __syncthreads();
  }
  if(mode==1){ float h=acc+b2f(bias[col]); float t=tanhf(0.7978845608028654f*(h+0.044715f*h*h*h)); acc=0.5f*h*(1.f+t);}
  C[row*ldc+col]=f2b(acc);
}
__global__ void k_attn(const bf16* qkv, bf16* o){
  int q=blockIdx.x, h=blockIdx.y; int t=threadIdx.x;
  __shared__ float p[256]; __shared__ float red[256];
  int key=q-t; float s=-1e30f;
  if(key>=0){ s=0; for(int d=0;d<DH;d++) s+=b2f(qkv[q*3*D+h*DH+d])*b2f(qkv[key*3*D+D+h*DH+d]); s*=rsqrtf((float)DH);}
  red[t]=s; __syncthreads();
  for(int o2=128;o2;o2>>=1){ if(t<o2) red[t]=fmaxf(red[t],red[t+o2]); __syncthreads(); }
  float m=red[0]; __syncthreads();
  float e=key>=0?__expf(s-m):0.f; p[t]=e; red[t]=e; __syncthreads();
  for(int o2=128;o2;o2>>=1){ if(t<o2) red[t]+=red[t+o2]; __syncthreads(); }
  float l=red[0];
  if(t<DH){ float a=0; for(int j=0;j<256;j++){int kk=q-j; if(kk>=0) a+=p[j]*b2f(qkv[kk*3*D+2*D+h*DH+t]);} o[q*2240+h*DH+t]=f2b(a/l);}
}
__global__ void k_final(const bf16* oh,const bf16* wo,const bf16* w2,const bf16* b2,const bf16* x,bf16* out){
  __shared__ float As[16][17], Bs[16][17];
  int tx=threadIdx.x&15, ty=threadIdx.x>>4;
  int row=blockIdx.y*16+ty, col=blockIdx.x*16+tx;
  float acc=0;
  for(int k0=0;k0<2240;k0+=16){
    As[ty][tx]=b2f(oh[row*2240+k0+tx]);
    int k=k0+ty;
    Bs[ty][tx]= k<448? b2f(wo[k*D+col]) : b2f(w2[(k-448)*D+col]);
    __syncthreads();
    for(int kk=0;kk<16;kk++) acc+=As[ty][kk]*Bs[kk][tx];
    __syncthreads();
  }
  out[row*D+col]=f2b(acc+b2f(b2[col])+b2f(x[row*D+col]));
}

#define RT() __builtin_amdgcn_s_memrealtime()
__global__ void k_delay(const unsigned long long* ts,int sel){
  unsigned long long mn=~0ull,mx=0,sum=0,md=0,me=0;
  for(int b=0;b<256;b++){ unsigned long long s=ts[b*2],e=ts[b*2+1]; mn=s<mn?s:mn; mx=s>mx?s:mx; sum+=e-s; md=(e-s)>md?(e-s):md; me=e>me?e:me; }
  unsigned long long v= sel==0? mx-mn : sel==1? sum/256 : sel==2? md : me-mx;
  if(v>3000) v=3000;
  unsigned long long t0=RT();
  while(RT()-t0 < v*100) __builtin_amdgcn_s_sleep(8);
}

__global__ __launch_bounds__(512) void k_probe(unsigned long long* ts){ unsigned long long t0=RT(); if(threadIdx.x==0){ts[blockIdx.x*2]=t0; ts[blockIdx.x*2+1]=t0;}
  while(RT()-t0<400) __builtin_amdgcn_s_sleep(2); }

extern "C" void kernel_launch(void* const* d_in, const int* in_sizes, int n_in, void* d_out, int out_size, void* d_ws, size_t ws_size, hipStream_t stream){
  const bf16* x=(const bf16*)d_in[0]; const bf16* wqkv=(const bf16*)d_in[1]; const bf16* wo=(const bf16*)d_in[2];
  const bf16* w1=(const bf16*)d_in[4]; const bf16* b1=(const bf16*)d_in[5]; const bf16* w2=(const bf16*)d_in[6]; const bf16* b2=(const bf16*)d_in[7];
  bf16* xn=(bf16*)d_ws; bf16* qkv=xn+S*D; bf16* oh=qkv+S*3*D;
  unsigned long long* ts=(unsigned long long*)((char*)d_ws+(128<<20));
  k_probe<<<256,512,65536,stream>>>(ts);
  k_ln<<<S,64,0,stream>>>(x,xn);
  k_gemm<<<dim3(3*D/16,S/16),256,0,stream>>>(xn,D,wqkv,3*D,qkv,3*D,D,nullptr,0);
  k_gemm<<<dim3(F/16,S/16),256,0,stream>>>(xn,D,w1,F,oh+448,2240,D,b1,1);
  k_attn<<<dim3(S,H),256,0,stream>>>(qkv,oh);
  k_final<<<dim3(D/16,S/16),256,0,stream>>>(oh,wo,w2,b2,x,(bf16*)d_out);
  k_delay<<<1,1,0,stream>>>(ts,0);
}
```

```cpp
#include <hip/hip_runtime.h>
#include <hip/hip_bf16.h>

typedef __bf16 bf16x2 __attribute__((ext_vector_type(2)));
typedef __bf16 bf16x4 __attribute__((ext_vector_type(4)));
typedef __bf16 bf16x8 __attribute__((ext_vector_type(8)));
typedef float f32x2 __attribute__((ext_vector_type(2)));
typedef float f32x4 __attribute__((ext_vector_type(4)));
typedef unsigned u32x2 __attribute__((ext_vector_type(2)));
typedef unsigned u32x4 __attribute__((ext_vector_type(4)));
typedef int i32x4 __attribute__((ext_vector_type(4)));
typedef unsigned long long u64;
#define LDSP(T) __attribute__((address_space(3))) T*
#define GLBP(T) __attribute__((address_space(1))) T*
#define DI __device__ __forceinline__
#define MFMA(a, b, c) __builtin_amdgcn_mfma_f32_16x16x32_bf16((a), (b), (c), 0, 0, 0)

constexpr unsigned WS_EPOCH = 0;
constexpr unsigned FLAG_STRIDE = 128;
constexpr unsigned WS_FRP = 4096;
constexpr unsigned WS_FKV = WS_FRP + 256 * FLAG_STRIDE;
constexpr unsigned WS_FH = WS_FKV + 1024 * FLAG_STRIDE;
constexpr unsigned WS_FO = WS_FH + 1024 * FLAG_STRIDE;
constexpr unsigned WS_FP = WS_FO + 1024 * FLAG_STRIDE;
constexpr unsigned WS_WQ = 1u << 20;
constexpr unsigned WQ_HEAD = 90 * 1024;
constexpr unsigned WS_H = 3u << 20;
constexpr unsigned WS_KV = 11u << 20;
constexpr unsigned WS_O = 15u << 20;
constexpr unsigned WS_P = 17u << 20;
constexpr unsigned WS_DIAG = 22u << 20;
constexpr unsigned WS_BYTES = 24u << 20;

constexpr unsigned SL = 16384;
constexpr unsigned L_MISC = 9 * SL;
constexpr unsigned L_CNT = L_MISC;
constexpr unsigned L_TAG = L_MISC + 64;
constexpr unsigned L_RSTD = L_MISC + 128;
constexpr unsigned L_NMR = L_RSTD + 512;
constexpr unsigned L_CS = L_NMR + 512;
constexpr unsigned L_B1 = L_CS + 512;
constexpr unsigned L_WQ = 4 * SL;
enum { C_A1 = 0, C_A2, C_A3, C_A4, C_A5, C_W2A, C_W2B, C_F1, C_F2, C_WO };

constexpr int SPIN_G = 20000;
constexpr int SPIN_L = 400000;

#define TS(n) do { } while (0)

template <class T> DI T lds_ld(unsigned a) { return *(LDSP(T))(size_t)a; }
template <class T> DI void lds_st(unsigned a, T v) { *(LDSP(T))(size_t)a = v; }
DI bf16x8 tr2(unsigned a0, unsigned a1) {
  bf16x4 lo = __builtin_amdgcn_ds_read_tr16_b64_v4bf16((LDSP(bf16x4))(size_t)a0);
  bf16x4 hi = __builtin_amdgcn_ds_read_tr16_b64_v4bf16((LDSP(bf16x4))(size_t)a1);
  return __builtin_shufflevector(lo, hi, 0, 1, 2, 3, 4, 5, 6, 7);
}
DI unsigned pk2(float a, float b) { f32x2 v = {a, b}; return __builtin_bit_cast(unsigned, __builtin_convertvector(v, bf16x2)); }
DI u32x4 pk8(f32x4 a, f32x4 b) { return u32x4{pk2(a[0], a[1]), pk2(a[2], a[3]), pk2(b[0], b[1]), pk2(b[2], b[3])}; }
DI bf16x8 asb(u32x4 v) { return __builtin_bit_cast(bf16x8, v); }
DI float bflo(unsigned u) { return __uint_as_float(u << 16); }
DI float bfhi(unsigned u) { return __uint_as_float(u & 0xffff0000u); }

DI i32x4 mksrd(const void* p, unsigned bytes) {
  i32x4 r; r[0] = (int)(size_t)p; r[1] = (int)(((size_t)p) >> 32) & 0xffff; r[2] = (int)bytes; r[3] = 0x00020000; return r;
}
DI void dma4(unsigned v0, unsigned v1, unsigned v2, unsigned v3, i32x4 rs, unsigned so, unsigned m) {
  asm volatile("s_mov_b32 m0, %4\n\ts_nop 0\n\t"
               "buffer_load_dwordx4 %0, %5, %6 offen lds\n\t"
               "buffer_load_dwordx4 %1, %5, %6 offen offset:1024 lds\n\t"
               "buffer_load_dwordx4 %2, %5, %6 offen offset:2048 lds\n\t"
               "buffer_load_dwordx4 %3, %5, %6 offen offset:3072 lds"
               ::"v"(v0), "v"(v1), "v"(v2), "v"(v3), "s"(m), "s"(rs), "s"(so) : "memory");
}
DI void dma3(unsigned v0, unsigned v1, unsigned v2, i32x4 rs, unsigned so, unsigned m) {
  asm volatile("s_mov_b32 m0, %3\n\ts_nop 0\n\t"
               "buffer_load_dwordx4 %0, %4, %5 offen lds\n\t"
               "buffer_load_dwordx4 %1, %4, %5 offen offset:1024 lds\n\t"
               "buffer_load_dwordx4 %2, %4, %5 offen offset:2048 lds"
               ::"v"(v0), "v"(v1), "v"(v2), "s"(m), "s"(rs), "s"(so) : "memory");
}
DI void dma2(unsigned v0, unsigned v1, i32x4 rs, unsigned so, unsigned m) {
  asm volatile("s_mov_b32 m0, %2\n\ts_nop 0\n\t"
               "buffer_load_dwordx4 %0, %3, %4 offen lds\n\t"
               "buffer_load_dwordx4 %1, %3, %4 offen offset:1024 lds"
               ::"v"(v0), "v"(v1), "s"(m), "s"(rs), "s"(so) : "memory");
}
DI void dma4c(unsigned v, i32x4 rs, unsigned so, unsigned m) {
  asm volatile("s_mov_b32 m0, %1\n\ts_nop 0\n\t"
               "buffer_load_dwordx4 %0, %2, %3 offen sc1 lds\n\t"
               "buffer_load_dwordx4 %0, %2, %3 offen offset:1024 sc1 lds\n\t"
               "buffer_load_dwordx4 %0, %2, %3 offen offset:2048 sc1 lds\n\t"
               "buffer_load_dwordx4 %0, %2, %3 offen offset:3072 sc1 lds"
               ::"v"(v), "s"(m), "s"(rs), "s"(so) : "memory");
}
DI void dma2c(unsigned v, i32x4 rs, unsigned so, unsigned m) {
  asm volatile("s_mov_b32 m0, %1\n\ts_nop 0\n\t"
               "buffer_load_dwordx4 %0, %2, %3 offen sc1 lds\n\t"
               "buffer_load_dwordx4 %0, %2, %3 offen offset:1024 sc1 lds"
               ::"v"(v), "s"(m), "s"(rs), "s"(so) : "memory");
}
#define VMCNT(n) asm volatile("s_waitcnt vmcnt(" #n ")" ::: "memory")
#define LGKM0() do { asm volatile("" ::: "memory"); __builtin_amdgcn_s_waitcnt(0xc07f); asm volatile("" ::: "memory"); } while (0)
#define BARRIER() do { LGKM0(); asm volatile("s_barrier" ::: "memory"); } while (0)
#define SCHED() __builtin_amdgcn_sched_barrier(0)

DI void lsignal(int c, int lane) {
  LGKM0();
  if (lane == 0) __hip_atomic_fetch_add((LDSP(unsigned))(size_t)(L_CNT + 4 * c), 1u, __ATOMIC_RELAXED, __HIP_MEMORY_SCOPE_WORKGROUP);
  asm volatile("" ::: "memory");
}
DI void lwait(int c) {
  for (int s = 0; s < SPIN_L; ++s) {
    unsigned v = __hip_atomic_load((LDSP(unsigned))(size_t)(L_CNT + 4 * c), __ATOMIC_RELAXED, __HIP_MEMORY_SCOPE_WORKGROUP);
    if (__builtin_amdgcn_readfirstlane(v) >= 4u) break;
  }
  asm volatile("" ::: "memory");
}
DI void gflag(char* ws, unsigned off, u64 tag, int lane) {
  if (lane == 0) __hip_atomic_store((GLBP(u64))(ws + off), tag, __ATOMIC_RELAXED, __HIP_MEMORY_SCOPE_AGENT);
}
DI void gpoll(char* ws, unsigned off, bool act, u64 tag) {
  for (int s = 0; s < SPIN_G; ++s) {
    u64 v = tag;
    if (act) v = __hip_atomic_load((GLBP(u64))(ws + off), __ATOMIC_RELAXED, __HIP_MEMORY_SCOPE_AGENT);
    if (__all(v == tag)) break;
  }
  asm volatile("" ::: "memory");
}
DI float red_g_max(float m) {
  auto r = __builtin_amdgcn_permlane16_swap(__float_as_uint(m), __float_as_uint(m), false, false);
  m = fmaxf(__uint_as_float(r[0]), __uint_as_float(r[1]));
  auto q = __builtin_amdgcn_permlane32_swap(__float_as_uint(m), __float_as_uint(m), false, false);
  return fmaxf(__uint_as_float(q[0]), __uint_as_float(q[1]));
}
DI float red_g_sum(float m) {
  auto r = __builtin_amdgcn_permlane16_swap(__float_as_uint(m), __float_as_uint(m), false, false);
  m = __uint_as_float(r[0]) + __uint_as_float(r[1]);
  auto q = __builtin_amdgcn_permlane32_swap(__float_as_uint(m), __float_as_uint(m), false, false);
  return __uint_as_float(q[0]) + __uint_as_float(q[1]);
}
DI unsigned slot_of(int u) { return (unsigned)((u + 4) % 9) * SL; }

__global__ __launch_bounds__(512, 2) void fused_block(const char* __restrict__ x, const char* __restrict__ wqkv,
                                                      const char* __restrict__ wo, const char* __restrict__ w1,
                                                      const char* __restrict__ b1, const char* __restrict__ w2,
                                                      const char* __restrict__ b2, char* __restrict__ out, char* ws) {
  __shared__ __attribute__((aligned(1024))) char smem[163840];
  asm volatile("" ::"v"(smem) : "memory");
  TS(0);
  const int lane = threadIdx.x & 63;
  const int wave = __builtin_amdgcn_readfirstlane(threadIdx.x >> 6);
  const int w = wave & 3;
  const int b = blockIdx.x;
  const int jh = b & 1, i = ((b >> 1) & 3) * 4 + (b >> 6), jj = (b >> 3) & 7;
  const int head = 8 * jh + jj, j = 7 * jh + jj;
  const bool isF = jj < 7;
  const int g = lane >> 4, n16 = lane & 15, q4 = (lane >> 2) & 3, pp = lane & 3;
  const f32x4 zero4 = {0.f, 0.f, 0.f, 0.f};
  const u32x4 ones_u = {0x3f803f80u, 0x3f803f80u, 0x3f803f80u, 0x3f803f80u};
  const __amdgpu_buffer_rsrc_t wsr = __builtin_amdgcn_make_buffer_rsrc(ws, 0, WS_BYTES, 0x00020000);

  if (wave >= 4) {
    const i32x4 xs = mksrd(x, 2048 * 896), w1s = mksrd(w1, 448 * 3584), w2s = mksrd(w2, 1792 * 896),
                wos = mksrd(wo, 448 * 896), wss = mksrd(ws, WS_BYTES);
    unsigned vx[4], vw[4];
#pragma unroll
    for (int t = 0; t < 4; ++t) {
      vx[t] = (unsigned)((lane >> 3) * 896 + (((lane & 7) ^ ((4 * t + g) & 7)) << 4) + t * (7168 - 1024));
      vw[t] = (unsigned)(g * 3584 + ((n16 ^ (2 * (g | ((t >> 1) << 2)))) << 4) + t * (4 * 3584 - 1024));
    }
    const unsigned sx = (unsigned)((128 * i + 32 * w) * 896), sw = (unsigned)(16 * w * 3584 + 256 * j);
    const unsigned mw = (unsigned)(w * 4096);
#define LOAD_U(u) do { if (((u) & 1) == 0) dma4(vx[0], vx[1], vx[2], vx[3], xs, sx + 128 * ((u) >> 1), slot_of(u) + mw); \
      else if (isF) dma4(vw[0], vw[1], vw[2], vw[3], w1s, sw + 64 * 3584 * ((u) >> 1), slot_of(u) + mw); } while (0)
#pragma unroll
    for (int u = 0; u < 9; ++u) LOAD_U(u);

    bf16x8 xf[2][14];
    float s1[2] = {0.f, 0.f}, s2[2] = {0.f, 0.f};
    const unsigned xa = (unsigned)((32 * w + n16) * 128), xsw = (unsigned)((n16 >> 1) & 7);
#pragma unroll
    for (int c = 0; c < 7; ++c) {
      if (c == 0) VMCNT(28); else if (c <= 3) VMCNT(20); else if (c == 4) VMCNT(16); else if (c == 5) VMCNT(8); else VMCNT(0);
      if (!isF) VMCNT(0);
      BARRIER();
      if (c == 0) TS(1);
      if (c == 1) { LOAD_U(9); LOAD_U(10); }
      if (c == 2) { LOAD_U(11); LOAD_U(12); }
      if (c == 3) { LOAD_U(13); }
#pragma unroll
      for (int rt = 0; rt < 2; ++rt)
#pragma unroll
        for (int ks = 0; ks < 2; ++ks) {
          bf16x8 f = lds_ld<bf16x8>(slot_of(2 * c) + xa + rt * 2048 + (((4 * ks + g) ^ xsw) << 4));
          xf[rt][2 * c + ks] = f;
          u32x4 fu = __builtin_bit_cast(u32x4, f);
#pragma unroll
          for (int e = 0; e < 4; ++e) {
            const unsigned fe = fu[e];
            const bf16x2 p = __builtin_bit_cast(bf16x2, fe);
            s1[rt] = __builtin_amdgcn_fdot2_f32_bf16(p, __builtin_bit_cast(bf16x2, 0x3f803f80u), s1[rt], false);
            s2[rt] = __builtin_amdgcn_fdot2_f32_bf16(p, p, s2[rt], false);
          }
        }
      asm volatile("" : "+v"(s1[0]), "+v"(s1[1]), "+v"(s2[0]), "+v"(s2[1]));
    }
    BARRIER();
    TS(2);
    const u64 tag = lds_ld<u64>(L_TAG);
    {
      const int s = lane / 3, p = lane - 3 * s;
      const bool act = lane < 42 && ((s & 3) == w || w == 2);
      gpoll(ws, WS_FRP + (unsigned)((p * 14 + s) * 4 + (head >> 2)) * FLAG_STRIDE, act, tag);
    }
    TS(3);
    const unsigned vl = (unsigned)(lane * 16);
    const unsigned wq0 = WS_WQ + (unsigned)head * WQ_HEAD;
#pragma unroll
    for (int t = 0; t < 4; ++t)
      if (t < 3 || w < 3) dma4c(vl, wss, wq0 + (unsigned)((4 * t + w) * 4096), (unsigned)((4 * t + w) * 4096));
#pragma unroll
    for (int t = 0; t < 4; ++t)
      if (t < 3 || w < 3) dma2c(vl, wss, wq0 + 61440 + (unsigned)((4 * t + w) * 2048), L_WQ + (unsigned)((4 * t + w) * 2048));
    unsigned v2[4];
#pragma unroll
    for (int t = 0; t < 4; ++t) v2[t] = (unsigned)((lane >> 3) * 896 + (((lane & 7) ^ (2 * g)) << 4) + t * (7168 - 1024));
    const unsigned s2b = (unsigned)((896 * jh + 32 * w) * 896 + 128 * jj);
#define LOAD_W2(n, slot) do { int sl_ = jj + (n); if (sl_ >= 7) sl_ -= 7; \
      dma4(v2[0], v2[1], v2[2], v2[3], w2s, s2b + (unsigned)(sl_ * 128 * 896), (slot) * SL + mw); } while (0)
    if (isF) { LOAD_W2(0, 6); LOAD_W2(1, 7); LOAD_W2(2, 8); }

    float rstd[2]; unsigned nm2[2];
#pragma unroll
    for (int rt = 0; rt < 2; ++rt) {
      const float mean = red_g_sum(s1[rt]) * (1.f / 448.f);
      const float var = fmaxf(red_g_sum(s2[rt]) * (1.f / 448.f) - mean * mean, 0.f);
      rstd[rt] = __builtin_amdgcn_rsqf(var + 1e-5f);
      nm2[rt] = pk2(-mean, -mean);
      if (g == 0) {
        lds_st<float>(L_RSTD + (unsigned)((32 * w + 16 * rt + n16) * 4), rstd[rt]);
        lds_st<float>(L_NMR + (unsigned)((32 * w + 16 * rt + n16) * 4), -mean * rstd[rt]);
      }
    }
    BARRIER();
    bf16x8 xaug[2];
#pragma unroll
    for (int rt = 0; rt < 2; ++rt) { const unsigned z = g == 3 ? 0u : nm2[rt]; xaug[rt] = asb(u32x4{nm2[rt], nm2[rt], z, z}); }
    f32x4 rsv[2];
#pragma unroll
    for (int rt = 0; rt < 2; ++rt) rsv[rt] = lds_ld<f32x4>(L_RSTD + (unsigned)((32 * w + 16 * rt + 4 * g) * 4));

    if (isF) VMCNT(18); else VMCNT(6);
    lsignal(C_A1, lane); lwait(C_A1);
    TS(4);
    f32x4 ak[2][2] = {{zero4, zero4}, {zero4, zero4}}, av[2][2] = {{zero4, zero4}, {zero4, zero4}};
#pragma unroll
    for (int s = 0; s < 15; ++s) {
      u32x4 wf[4];
#pragma unroll
      for (int t = 0; t < 4; ++t) {
        wf[t] = lds_ld<u32x4>(vl + (unsigned)(s * 4096 + t * 1024));
        if (s == 14 && g == 3) { wf[t][2] = 0u; wf[t][3] = 0u; }
      }
#pragma unroll
      for (int rt = 0; rt < 2; ++rt) {
        const bf16x8 xx = s < 14 ? xf[rt][s < 14 ? s : 0] : xaug[rt];
        ak[rt][0] = MFMA(asb(wf[0]), xx, ak[rt][0]);
        ak[rt][1] = MFMA(asb(wf[1]), xx, ak[rt][1]);
        av[rt][0] = MFMA(xx, asb(wf[2]), av[rt][0]);
        av[rt][1] = MFMA(xx, asb(wf[3]), av[rt][1]);
      }
    }
    VMCNT(0);
    lsignal(C_A2, lane);
    if (isF) lsignal(C_W2A, lane);
    u32x4 kvf[4];
    kvf[0] = pk8(ak[0][0] * rstd[0], ak[0][1] * rstd[0]);
    kvf[1] = pk8(ak[1][0] * rstd[1], ak[1][1] * rstd[1]);
    kvf[2] = pk8(av[0][0] * rsv[0], av[1][0] * rsv[1]);
    kvf[3] = pk8(av[0][1] * rsv[0], av[1][1] * rsv[1]);
    if (n16 == 12) kvf[3] = ones_u;
    const unsigned kvo = WS_KV + (unsigned)((i * 16 + head) * 16384 + w * 4096);
#pragma unroll
    for (int t = 0; t < 4; ++t) __builtin_amdgcn_raw_buffer_store_b128(kvf[t], wsr, vl + t * 1024, kvo, 16);
    lwait(C_A2);
#pragma unroll
    for (int t = 0; t < 4; ++t) lds_st<u32x4>(32768 + mw + vl + t * 1024, kvf[t]);
    TS(5);
    f32x4 aq[2][2] = {{zero4, zero4}, {zero4, zero4}};
#pragma unroll
    for (int s = 0; s < 15; ++s) {
      u32x4 wf[2];
#pragma unroll
      for (int t = 0; t < 2; ++t) {
        wf[t] = lds_ld<u32x4>(L_WQ + vl + (unsigned)(s * 2048 + t * 1024));
        if (s == 14 && g == 3) { wf[t][2] = 0u; wf[t][3] = 0u; }
      }
#pragma unroll
      for (int rt = 0; rt < 2; ++rt) {
        const bf16x8 xx = s < 14 ? xf[rt][s < 14 ? s : 0] : xaug[rt];
        aq[rt][0] = MFMA(asb(wf[0]), xx, aq[rt][0]);
        aq[rt][1] = MFMA(asb(wf[1]), xx, aq[rt][1]);
      }
    }
    VMCNT(0);
    gflag(ws, WS_FKV + (unsigned)((i * 16 + head) * 4 + w) * FLAG_STRIDE, tag, lane);
    lsignal(C_A3, lane);
    bf16x8 qf[2];
#pragma unroll
    for (int rt = 0; rt < 2; ++rt) {
      const float sc = rstd[rt] * (0.18898223650461362f * 1.4426950408889634f);
      u32x4 u = pk8(aq[rt][0] * sc, aq[rt][1] * sc);
      if (g == 3) u[2] = 0x00003f80u;
      qf[rt] = asb(u);
    }
    TS(6);
    if (isF) {
      lwait(C_A3); lwait(C_F1);
      LOAD_W2(3, 3); LOAD_W2(4, 4); LOAD_W2(5, 5); LOAD_W2(6, 6);
    }
    gpoll(ws, WS_FKV + (unsigned)(((i - 1 - (lane & 1)) * 16 + head) * 4 + w) * FLAG_STRIDE, lane < 2 && lane < i, tag);
    TS(7);
#pragma unroll
    for (int d = 1; d <= 2; ++d) {
      const unsigned dst = (unsigned)((2 - d) * 16384) + mw;
      if (i >= d) dma4c(vl, wss, kvo - (unsigned)(d * 16 * 16384), dst);
      else {
        const u32x4 kz = {0u, 0u, g == 3 ? 0x0000f149u : 0u, 0u}, vz = {0u, 0u, 0u, 0u};
        lds_st<u32x4>(dst + vl, kz); lds_st<u32x4>(dst + vl + 1024, kz);
        lds_st<u32x4>(dst + vl + 2048, vz); lds_st<u32x4>(dst + vl + 3072, vz);
      }
    }
    VMCNT(0);
    if (isF) lsignal(C_W2B, lane);
    lsignal(C_A4, lane); lwait(C_A4);
    TS(8);
    f32x4 cfirst, cdiag;
#pragma unroll
    for (int r = 0; r < 4; ++r) { cfirst[r] = (4 * g + r > n16) ? 0.f : -1e30f; cdiag[r] = (4 * g + r <= n16) ? 0.f : -1e30f; }
    const unsigned kb = mw + vl;
    f32x4 sa[18], sb[18];
#pragma unroll
    for (int t = 0; t < 18; ++t) {
      const bf16x8 kf = lds_ld<bf16x8>(kb + (unsigned)((t >> 1) * 4096 + (t & 1) * 1024));
      if (t < 17) sa[t] = MFMA(kf, qf[0], t == 0 ? cfirst : t == 16 ? cdiag : zero4);
      if (t > 0) sb[t] = MFMA(kf, qf[1], t == 1 ? cfirst : t == 17 ? cdiag : zero4);
    }
    float ma = sa[0][0], mb = sb[1][0];
#pragma unroll
    for (int t = 0; t < 17; ++t) {
      ma = fmaxf(fmaxf(ma, sa[t][0]), sa[t][1]); ma = fmaxf(fmaxf(ma, sa[t][2]), sa[t][3]);
      mb = fmaxf(fmaxf(mb, sb[t + 1][0]), sb[t + 1][1]); mb = fmaxf(fmaxf(mb, sb[t + 1][2]), sb[t + 1][3]);
    }
    ma = red_g_max(ma); mb = red_g_max(mb);
    f32x4 oa[2] = {zero4, zero4}, ob[2] = {zero4, zero4};
#pragma unroll
    for (int ks = 0; ks < 9; ++ks) {
      f32x4 pa[2], pb[2];
#pragma unroll
      for (int e = 0; e < 2; ++e) {
        const int t = 2 * ks + e;
#pragma unroll
        for (int r = 0; r < 4; ++r) {
          pa[e][r] = t < 17 ? __builtin_amdgcn_exp2f(sa[t][r] - ma) : 0.f;
          pb[e][r] = t > 0 ? __builtin_amdgcn_exp2f(sb[t][r] - mb) : 0.f;
        }
      }
      const bf16x8 pfa = asb(pk8(pa[0], pa[1])), pfb = asb(pk8(pb[0], pb[1]));
#pragma unroll
      for (int dt = 0; dt < 2; ++dt) {
        const bf16x8 vf = lds_ld<bf16x8>(kb + (unsigned)(ks * 4096 + 2048 + dt * 1024));
        oa[dt] = MFMA(vf, pfa, oa[dt]);
        ob[dt] = MFMA(vf, pfb, ob[dt]);
      }
    }
    lsignal(C_A5, lane);
    {
      const float la = __shfl(oa[1][0], 48 + n16), lb = __shfl(ob[1][0], 48 + n16);
      const float ra = __builtin_amdgcn_rcpf(la), rb = __builtin_amdgcn_rcpf(lb);
      u32x4 ua = pk8(oa[0] * ra, oa[1] * ra), ub = pk8(ob[0] * rb, ob[1] * rb);
      if (g == 3) { ua[2] = 0u; ua[3] = 0u; ub[2] = 0u; ub[3] = 0u; }
      const unsigned oo = WS_O + (unsigned)(((8 * i + 2 * w) * 16 + head) * 1024);
      __builtin_amdgcn_raw_buffer_store_b128(ua, wsr, vl, oo, 16);
      __builtin_amdgcn_raw_buffer_store_b128(ub, wsr, vl, oo + 16384, 16);
    }
    TS(9);
    VMCNT(0);
    gflag(ws, WS_FO + (unsigned)((i * 16 + head) * 4 + w) * FLAG_STRIDE, tag, lane);
    TS(10);
    if (isF) {
      lwait(C_A5); lwait(C_F2);
      const unsigned so = (unsigned)(112 * w * 896 + 128 * jj), mo = (unsigned)(w * 14336);
      dma4(v2[0], v2[1], v2[2], v2[3], wos, so, mo);
      dma4(v2[0], v2[1], v2[2], v2[3], wos, so + 32 * 896, mo + 4096);
      dma4(v2[0], v2[1], v2[2], v2[3], wos, so + 64 * 896, mo + 8192);
      dma2(v2[0], v2[1], wos, so + 96 * 896, mo + 12288);
      VMCNT(0);
      lsignal(C_WO, lane);
    }
    TS(11);
  } else if (!isF) {
    const i32x4 wqs = mksrd(wqkv, 448 * 2688);
    const int fw = ((i * 2 + jh) << 2) + w;
    const unsigned stg = slot_of(2 * w + 1);
    unsigned vr[7];
#pragma unroll
    for (int t = 0; t < 7; ++t) { const int c = 64 * t + lane, r = c / 14; vr[t] = (unsigned)(r * 2688 + (c - 14 * r) * 16 - (t & 3) * 1024); }
    const u64 e0 = __hip_atomic_load((GLBP(u64))(ws + WS_EPOCH), __ATOMIC_RELAXED, __HIP_MEMORY_SCOPE_AGENT);
    const int nun = fw < 40 ? 2 : 1;
    for (int r = 0; r < nun; ++r) {
      const int un = fw + 128 * r, po = un / 56, rem = un - 56 * po, s = rem >> 2, qd = rem & 3, p = po == 2 ? 0 : po + 1;
      const unsigned so = (unsigned)(32 * s * 2688 + p * 896 + qd * 224);
      dma4(vr[0], vr[1], vr[2], vr[3], wqs, so, stg + r * 7168);
      dma3(vr[4], vr[5], vr[6], wqs, so, stg + r * 7168 + 4096);
    }
    const u64 tag = e0 * 6364136223846793005ull + 1442695040888963407ull;
    if (wave == 0) {
      if (lane < 16) lds_st<unsigned>(L_CNT + 4 * lane, 0u);
      if (lane == 0) lds_st<u64>(L_TAG, tag);
    }
    VMCNT(0);
    TS(1);
    for (int r = 0; r < nun; ++r) {
      const int un = fw + 128 * r, po = un / 56, rem = un - 56 * po, s = rem >> 2, qd = rem & 3, p = po == 2 ? 0 : po + 1;
      const unsigned ra = stg + r * 7168 + (unsigned)((8 * g + q4) * 224 + pp * 8);
      const unsigned d0 = WS_WQ + (unsigned)(4 * qd) * WQ_HEAD + (p == 0 ? 61440u : (unsigned)((p - 1) * 2048));
      const unsigned kst = p == 0 ? 2048u : 4096u;
      float cs[2] = {0.f, 0.f};
#pragma unroll
      for (int hl = 0; hl < 4; ++hl)
#pragma unroll
        for (int tl = 0; tl < 2; ++tl) {
          u32x4 f = __builtin_bit_cast(u32x4, tr2(ra + hl * 56 + tl * 32, ra + hl * 56 + tl * 32 + 4 * 224));
          if (tl == 1 && n16 >= 12) f = u32x4{0u, 0u, 0u, 0u};
          __builtin_amdgcn_raw_buffer_store_b128(f, wsr, lane * 16 + tl * 1024, d0 + hl * WQ_HEAD + s * kst, 16);
          const f32x4 d = MFMA(asb(ones_u), asb(f), zero4);
          if (g == hl) cs[tl] = d[0];
        }
#pragma unroll
      for (int tl = 0; tl < 2; ++tl) {
        const unsigned hi = pk2(cs[tl], 0.f) & 0xffffu;
        const unsigned v = hi | (pk2(cs[tl] - bflo(hi), 0.f) << 16);
        __builtin_amdgcn_raw_buffer_store_b32(v, wsr, g * WQ_HEAD + n16 * 16 + tl * 1024,
                                              d0 + 14 * kst + (unsigned)((s >> 2) * 256 + (s & 3) * 4), 16);
      }
      VMCNT(0);
      gflag(ws, WS_FRP + (unsigned)((p * 14 + s) * 4 + qd) * FLAG_STRIDE, tag, lane);
    }
    TS(2);
    for (int c = 0; c < 9; ++c) BARRIER();
    if (b == 56 && w == 0) {
      for (int r = 0; r < 4; ++r) gpoll(ws, WS_FKV + (unsigned)((r * 64 + lane) * 4) * FLAG_STRIDE, true, tag);
      gflag(ws, WS_EPOCH, tag, lane);
    }
    TS(3);
  } else {
    if (wave == 0) {
      const u64 e0 = __hip_atomic_load((GLBP(u64))(ws + WS_EPOCH), __ATOMIC_RELAXED, __HIP_MEMORY_SCOPE_AGENT);
      if (lane < 16) lds_st<unsigned>(L_CNT + 4 * lane, 0u);
      if (lane == 0) lds_st<u64>(L_TAG, e0 * 6364136223846793005ull + 1442695040888963407ull);
    } else if (wave == 1) {
      const unsigned u = *(const unsigned*)(b1 + 256 * j + 4 * lane);
      lds_st<f32x2>(L_B1 + 8 * lane, f32x2{bflo(u), bfhi(u)});
    }
    u32x2 b2v[4];
#pragma unroll
    for (int ct = 0; ct < 4; ++ct) b2v[ct] = *(const u32x2*)(b2 + 128 * jj + 32 * ct + 8 * g);

    f32x4 acc[2][8], accs[2] = {zero4, zero4};
#pragma unroll
    for (int ct = 0; ct < 8; ++ct) { acc[0][ct] = zero4; acc[1][ct] = zero4; }
    u32x2 xres[4];
#pragma unroll
    for (int ct = 0; ct < 4; ++ct) xres[ct] = u32x2{0u, 0u};
    const unsigned xa = (unsigned)((16 * w + n16) * 128), xsw = (unsigned)((n16 >> 1) & 7);
    const unsigned wa = (unsigned)((8 * g + q4) * 256 + pp * 8 + (((q4 | ((g & 1) << 2)) ^ (2 * w)) << 5));
    bf16x8 xA[2], wA[8], xB[2], wB[8];
#define F_LOAD(X, W, xs_, ws_, ks) do { \
      X[0] = lds_ld<bf16x8>((xs_) + xa + (((4 * (ks) + g) ^ xsw) << 4)); \
      X[1] = lds_ld<bf16x8>((xs_) + xa + 8192 + (((4 * (ks) + g) ^ xsw) << 4)); \
      _Pragma("unroll") for (int t = 0; t < 8; ++t) { const unsigned a_ = (ws_) + (wa ^ (unsigned)(t << 5)) + (ks) * 8192; W[t] = tr2(a_, a_ + 1024); } } while (0)
#define F_MMA(X, W) do { \
      _Pragma("unroll") for (int t = 0; t < 8; ++t) { acc[0][t] = MFMA(W[t], X[0], acc[0][t]); acc[1][t] = MFMA(W[t], X[1], acc[1][t]); } \
      accs[0] = MFMA(W[0], asb(ones_u), accs[0]); accs[1] = MFMA(W[1], asb(ones_u), accs[1]); } while (0)
    unsigned xsl = 4 * SL, wsl = 5 * SL;
    BARRIER();
    TS(1);
    F_LOAD(xA, wA, xsl, wsl, 0);
#pragma unroll 1
    for (int c = 0; c < 7; ++c) {
      F_LOAD(xB, wB, xsl, wsl, 1);
      if (c == jj) {
#pragma unroll
        for (int ct = 0; ct < 4; ++ct)
          xres[ct] = lds_ld<u32x2>(xsl + xa + (unsigned)(jh * 8192) + (((2 * ct + (g >> 1)) ^ xsw) << 4) + (g & 1) * 8);
      }
      SCHED();
      F_MMA(xA, wA);
      SCHED();
      BARRIER();
      xsl += 2 * SL; if (xsl >= 9 * SL) xsl -= 9 * SL;
      wsl += 2 * SL; if (wsl >= 9 * SL) wsl -= 9 * SL;
      if (c < 6) F_LOAD(xA, wA, xsl, wsl, 0);
      SCHED();
      F_MMA(xB, wB);
      SCHED();
    }
    if (n16 == 0) {
      lds_st<f32x4>(L_CS + (unsigned)((32 * w + 4 * g) * 4), accs[0]);
      lds_st<f32x4>(L_CS + (unsigned)((32 * w + 16 + 4 * g) * 4), accs[1]);
    }
    BARRIER();
    TS(2);
    const u64 tag = lds_ld<u64>(L_TAG);
    u32x4 hf[2][4];
    const unsigned vl = (unsigned)(lane * 16);
    {
      float rs[2], nm[2];
#pragma unroll
      for (int rt = 0; rt < 2; ++rt) {
        rs[rt] = lds_ld<float>(L_RSTD + (unsigned)((64 * rt + 16 * w + n16) * 4));
        nm[rt] = lds_ld<float>(L_NMR + (unsigned)((64 * rt + 16 * w + n16) * 4));
      }
#pragma unroll
      for (int kk = 0; kk < 4; ++kk) {
        f32x4 hv[2][2];
#pragma unroll
        for (int e = 0; e < 2; ++e) {
          const int t = 2 * kk + e;
          const unsigned co = (unsigned)(((t ^ (2 * w)) * 16 + 4 * g) * 4);
          const f32x4 cs = lds_ld<f32x4>(L_CS + co), bb = lds_ld<f32x4>(L_B1 + co);
#pragma unroll
          for (int rt = 0; rt < 2; ++rt) {
            const f32x4 v = acc[rt][t] * rs[rt] + (cs * nm[rt] + bb);
            const f32x4 z = v * (v * v * (-0.10294324f) + (-2.3022082f));
            f32x4 r;
#pragma unroll
            for (int q = 0; q < 4; ++q) r[q] = __builtin_amdgcn_rcpf(1.f + __builtin_amdgcn_exp2f(z[q]));
            hv[rt][e] = v * r;
          }
        }
#pragma unroll
        for (int rt = 0; rt < 2; ++rt) {
          hf[rt][kk] = pk8(hv[rt][0], hv[rt][1]);
          __builtin_amdgcn_raw_buffer_store_b128(hf[rt][kk], wsr, vl,
              WS_H + (unsigned)(((8 * i + 4 * rt + w) * 56 + 4 * j + (kk ^ w)) * 1024), 16);
        }
      }
    }
    TS(3);
    f32x4 oc[2][4];
#pragma unroll
    for (int ct = 0; ct < 4; ++ct) { oc[0][ct] = zero4; oc[1][ct] = zero4; }
    const unsigned w2a = (unsigned)((4 * g + q4) * 128 + pp * 8 + (((2 * g + (q4 >> 1)) & 3) << 5));
    VMCNT(0);
    gflag(ws, WS_FH + (unsigned)((i * 16 + j) * 4 + w) * FLAG_STRIDE, tag, lane);
    TS(14);
    lwait(C_W2A);
#pragma unroll
    for (int kk = 0; kk < 4; ++kk) {
      const unsigned base = 6 * SL + (unsigned)((kk ^ w) * 4096);
#pragma unroll
      for (int ct = 0; ct < 4; ++ct) {
        const unsigned a = base + (w2a ^ (unsigned)(ct << 5));
        const bf16x8 wf = tr2(a, a + 2048);
        oc[0][ct] = MFMA(wf, asb(hf[0][kk]), oc[0][ct]);
        oc[1][ct] = MFMA(wf, asb(hf[1][kk]), oc[1][ct]);
      }
    }
    lsignal(C_F1, lane);
    TS(4);
    gpoll(ws, WS_FH + (unsigned)((i * 16 + 7 * jh + (lane & 7)) * 4 + w) * FLAG_STRIDE, lane < 7 && lane != jj, tag);
    TS(5);
    const unsigned hb = WS_H + (unsigned)(((8 * i + w) * 56 + 28 * jh) * 1024);
    u32x4 hr[3][2][4];
#define H_LOAD(n) do { int sl_ = jj + (n); if (sl_ >= 7) sl_ -= 7; \
      _Pragma("unroll") for (int kk = 0; kk < 4; ++kk) _Pragma("unroll") for (int rt = 0; rt < 2; ++rt) \
        hr[((n) - 1) % 3][rt][kk] = __builtin_amdgcn_raw_buffer_load_b128(wsr, vl + kk * 1024, hb + (unsigned)(rt * 4 * 56 * 1024 + sl_ * 4096), 16); } while (0)
    H_LOAD(1); H_LOAD(2); H_LOAD(3);
#pragma unroll
    for (int n = 1; n < 7; ++n) {
      if (n == 3) lwait(C_W2B);
      const unsigned sbase = (unsigned)(n < 3 ? 6 + n : n) * SL;
#pragma unroll
      for (int kk = 0; kk < 4; ++kk) {
#pragma unroll
        for (int ct = 0; ct < 4; ++ct) {
          const unsigned a = sbase + kk * 4096 + (w2a ^ (unsigned)(ct << 5));
          const bf16x8 wf = tr2(a, a + 2048);
          oc[0][ct] = MFMA(wf, asb(hr[(n - 1) % 3][0][kk]), oc[0][ct]);
          oc[1][ct] = MFMA(wf, asb(hr[(n - 1) % 3][1][kk]), oc[1][ct]);
        }
      }
      if (n == 3) lsignal(C_F2, lane);
      if (n + 3 < 7) H_LOAD(n + 3);
    }
    TS(6);
    f32x4 own[4];
#pragma unroll
    for (int ct = 0; ct < 4; ++ct) {
      const f32x4 snd = jh ? oc[0][ct] : oc[1][ct];
      own[ct] = jh ? oc[1][ct] : oc[0][ct];
      __builtin_amdgcn_raw_buffer_store_b128(__builtin_bit_cast(u32x4, snd), wsr, vl + ct * 1024,
                                             WS_P + (unsigned)(((i * 16 + j) * 4 + w) * 4096), 16);
    }
    VMCNT(0);
    gflag(ws, WS_FP + (unsigned)((i * 16 + j) * 4 + w) * FLAG_STRIDE, tag, lane);
    TS(7);
    gpoll(ws, WS_FO + (unsigned)((i * 16 + n16) * 4 + ((4 * jh + w) >> 1)) * FLAG_STRIDE, lane < 16, tag);
    TS(8);
    u32x4 of[16];
#pragma unroll
    for (int h = 0; h < 16; ++h)
      of[h] = __builtin_amdgcn_raw_buffer_load_b128(wsr, vl + (h & 3) * 1024,
                                                    WS_O + (unsigned)(((8 * i + 4 * jh + w) * 16 + (h & ~3)) * 1024), 16);
    lwait(C_WO);
    TS(9);
#pragma unroll
    for (int h = 0; h < 16; ++h) {
#pragma unroll
      for (int ct = 0; ct < 4; ++ct) {
        const unsigned a = (unsigned)(h * 28 * 128) + (w2a ^ (unsigned)((ct ^ (2 * (h & 1))) << 5));
        own[ct] = MFMA(tr2(a, a + 2048), asb(of[h]), own[ct]);
      }
    }
    TS(10);
    const int jp = j + 7 - 14 * jh;
    gpoll(ws, WS_FP + (unsigned)((i * 16 + jp) * 4 + w) * FLAG_STRIDE, lane == 0, tag);
    TS(11);
    char* orow = out + (size_t)(128 * i + 64 * jh + 16 * w + n16) * 896 + 128 * jj + 8 * g;
#pragma unroll
    for (int ct = 0; ct < 4; ++ct) {
      const f32x4 pv = __builtin_bit_cast(f32x4, __builtin_amdgcn_raw_buffer_load_b128(
          wsr, vl + ct * 1024, WS_P + (unsigned)(((i * 16 + jp) * 4 + w) * 4096), 16));
      const f32x4 xr = {bflo(xres[ct][0]), bfhi(xres[ct][0]), bflo(xres[ct][1]), bfhi(xres[ct][1])};
      const f32x4 bb = {bflo(b2v[ct][0]), bfhi(b2v[ct][0]), bflo(b2v[ct][1]), bfhi(b2v[ct][1])};
      const f32x4 r = own[ct] + pv + xr + bb;
      *(u32x2*)(orow + 32 * ct) = u32x2{pk2(r[0], r[1]), pk2(r[2], r[3])};
    }
    TS(12);
  }
}

extern "C" void kernel_launch(void* const* d_in, const int* in_sizes, int n_in, void* d_out, int out_size, void* d_ws,
                              size_t ws_size, hipStream_t stream) {
  fused_block<<<256, 512, 0, stream>>>((const char*)d_in[0], (const char*)d_in[1], (const char*)d_in[2], (const char*)d_in[4],
                                       (const char*)d_in[5], (const char*)d_in[6], (const char*)d_in[7], (char*)d_out,
                                       (char*)d_ws);
}
```

```cpp
#define DIAG
#include <hip/hip_runtime.h>
#include <hip/hip_bf16.h>

typedef __bf16 bf16x2 __attribute__((ext_vector_type(2)));
typedef __bf16 bf16x4 __attribute__((ext_vector_type(4)));
typedef __bf16 bf16x8 __attribute__((ext_vector_type(8)));
typedef float f32x2 __attribute__((ext_vector_type(2)));
typedef float f32x4 __attribute__((ext_vector_type(4)));
typedef unsigned u32x2 __attribute__((ext_vector_type(2)));
typedef unsigned u32x4 __attribute__((ext_vector_type(4)));
typedef int i32x4 __attribute__((ext_vector_type(4)));
typedef unsigned long long u64;
#define LDSP(T) __attribute__((address_space(3))) T*
#define GLBP(T) __attribute__((address_space(1))) T*
#define DI __device__ __forceinline__
#define MFMA(a, b, c) __builtin_amdgcn_mfma_f32_16x16x32_bf16((a), (b), (c), 0, 0, 0)

constexpr unsigned WS_EPOCH = 0;
constexpr unsigned FLAG_STRIDE = 128;
constexpr unsigned WS_FRP = 4096;
constexpr unsigned WS_FKV = WS_FRP + 256 * FLAG_STRIDE;
constexpr unsigned WS_FH = WS_FKV + 1024 * FLAG_STRIDE;
constexpr unsigned WS_FO = WS_FH + 1024 * FLAG_STRIDE;
constexpr unsigned WS_FP = WS_FO + 1024 * FLAG_STRIDE;
constexpr unsigned WS_WQ = 1u << 20;
constexpr unsigned WQ_HEAD = 90 * 1024;
constexpr unsigned WS_H = 3u << 20;
constexpr unsigned WS_KV = 11u << 20;
constexpr unsigned WS_O = 15u << 20;
constexpr unsigned WS_P = 17u << 20;
constexpr unsigned WS_DIAG = 22u << 20;
constexpr unsigned WS_BYTES = 24u << 20;

constexpr unsigned SL = 16384;
constexpr unsigned L_MISC = 9 * SL;
constexpr unsigned L_CNT = L_MISC;
constexpr unsigned L_TAG = L_MISC + 64;
constexpr unsigned L_RSTD = L_MISC + 128;
constexpr unsigned L_NMR = L_RSTD + 512;
constexpr unsigned L_CS = L_NMR + 512;
constexpr unsigned L_B1 = L_CS + 512;
constexpr unsigned L_WQ = 4 * SL;
enum { C_A1 = 0, C_A2, C_A3, C_A4, C_A5, C_W2A, C_W2B, C_F1, C_F2, C_WO };

constexpr int SPIN_G = 20000;
constexpr int SPIN_L = 400000;

#define TS(n) do { ts[n] = __builtin_amdgcn_s_memrealtime(); } while (0)

template <class T> DI T lds_ld(unsigned a) { return *(LDSP(T))(size_t)a; }
template <class T> DI void lds_st(unsigned a, T v) { *(LDSP(T))(size_t)a = v; }
DI bf16x8 tr2(unsigned a0, unsigned a1) {
  bf16x4 lo = __builtin_amdgcn_ds_read_tr16_b64_v4bf16((LDSP(bf16x4))(size_t)a0);
  bf16x4 hi = __builtin_amdgcn_ds_read_tr16_b64_v4bf16((LDSP(bf16x4))(size_t)a1);
  return __builtin_shufflevector(lo, hi, 0, 1, 2, 3, 4, 5, 6, 7);
}
DI unsigned pk2(float a, float b) { f32x2 v = {a, b}; return __builtin_bit_cast(unsigned, __builtin_convertvector(v, bf16x2)); }
DI u32x4 pk8(f32x4 a, f32x4 b) { return u32x4{pk2(a[0], a[1]), pk2(a[2], a[3]), pk2(b[0], b[1]), pk2(b[2], b[3])}; }
DI bf16x8 asb(u32x4 v) { return __builtin_bit_cast(bf16x8, v); }
DI float bflo(unsigned u) { return __uint_as_float(u << 16); }
DI float bfhi(unsigned u) { return __uint_as_float(u & 0xffff0000u); }

DI i32x4 mksrd(const void* p, unsigned bytes) {
  i32x4 r; r[0] = (int)(size_t)p; r[1] = (int)(((size_t)p) >> 32) & 0xffff; r[2] = (int)bytes; r[3] = 0x00020000; return r;
}
DI void dma4(unsigned v0, unsigned v1, unsigned v2, unsigned v3, i32x4 rs, unsigned so, unsigned m) {
  asm volatile("s_mov_b32 m0, %4\n\ts_nop 0\n\t"
               "buffer_load_dwordx4 %0, %5, %6 offen lds\n\t"
               "buffer_load_dwordx4 %1, %5, %6 offen offset:1024 lds\n\t"
               "buffer_load_dwordx4 %2, %5, %6 offen offset:2048 lds\n\t"
               "buffer_load_dwordx4 %3, %5, %6 offen offset:3072 lds"
               ::"v"(v0), "v"(v1), "v"(v2), "v"(v3), "s"(m), "s"(rs), "s"(so) : "memory");
}
DI void dma3(unsigned v0, unsigned v1, unsigned v2, i32x4 rs, unsigned so, unsigned m) {
  asm volatile("s_mov_b32 m0, %3\n\ts_nop 0\n\t"
               "buffer_load_dwordx4 %0, %4, %5 offen lds\n\t"
               "buffer_load_dwordx4 %1, %4, %5 offen offset:1024 lds\n\t"
               "buffer_load_dwordx4 %2, %4, %5 offen offset:2048 lds"
               ::"v"(v0), "v"(v1), "v"(v2), "s"(m), "s"(rs), "s"(so) : "memory");
}
DI void dma2(unsigned v0, unsigned v1, i32x4 rs, unsigned so, unsigned m) {
  asm volatile("s_mov_b32 m0, %2\n\ts_nop 0\n\t"
               "buffer_load_dwordx4 %0, %3, %4 offen lds\n\t"
               "buffer_load_dwordx4 %1, %3, %4 offen offset:1024 lds"
               ::"v"(v0), "v"(v1), "s"(m), "s"(rs), "s"(so) : "memory");
}
DI void dma4c(unsigned v, i32x4 rs, unsigned so, unsigned m) {
  asm volatile("s_mov_b32 m0, %1\n\ts_nop 0\n\t"
               "buffer_load_dwordx4 %0, %2, %3 offen sc1 lds\n\t"
               "buffer_load_dwordx4 %0, %2, %3 offen offset:1024 sc1 lds\n\t"
               "buffer_load_dwordx4 %0, %2, %3 offen offset:2048 sc1 lds\n\t"
               "buffer_load_dwordx4 %0, %2, %3 offen offset:3072 sc1 lds"
               ::"v"(v), "s"(m), "s"(rs), "s"(so) : "memory");
}
DI void dma2c(unsigned v, i32x4 rs, unsigned so, unsigned m) {
  asm volatile("s_mov_b32 m0, %1\n\ts_nop 0\n\t"
               "buffer_load_dwordx4 %0, %2, %3 offen sc1 lds\n\t"
               "buffer_load_dwordx4 %0, %2, %3 offen offset:1024 sc1 lds"
               ::"v"(v), "s"(m), "s"(rs), "s"(so) : "memory");
}
#define VMCNT(n) asm volatile("s_waitcnt vmcnt(" #n ")" ::: "memory")
#define LGKM0() do { asm volatile("" ::: "memory"); __builtin_amdgcn_s_waitcnt(0xc07f); asm volatile("" ::: "memory"); } while (0)
#define BARRIER() do { LGKM0(); asm volatile("s_barrier" ::: "memory"); } while (0)
#define SCHED() __builtin_amdgcn_sched_barrier(0)

DI void lsignal(int c, int lane) {
  LGKM0();
  if (lane == 0) __hip_atomic_fetch_add((LDSP(unsigned))(size_t)(L_CNT + 4 * c), 1u, __ATOMIC_RELAXED, __HIP_MEMORY_SCOPE_WORKGROUP);
  asm volatile("" ::: "memory");
}
DI void lwait(int c) {
  for (int s = 0; s < SPIN_L; ++s) {
    unsigned v = __hip_atomic_load((LDSP(unsigned))(size_t)(L_CNT + 4 * c), __ATOMIC_RELAXED, __HIP_MEMORY_SCOPE_WORKGROUP);
    if (__builtin_amdgcn_readfirstlane(v) >= 4u) break;
  }
  asm volatile("" ::: "memory");
}
DI void gflag(char* ws, unsigned off, u64 tag, int lane) {
  if (lane == 0) __hip_atomic_store((GLBP(u64))(ws + off), tag, __ATOMIC_RELAXED, __HIP_MEMORY_SCOPE_AGENT);
}
DI void gpoll(char* ws, unsigned off, bool act, u64 tag) {
  for (int s = 0; s < SPIN_G; ++s) {
    u64 v = tag;
    if (act) v = __hip_atomic_load((GLBP(u64))(ws + off), __ATOMIC_RELAXED, __HIP_MEMORY_SCOPE_AGENT);
    if (__all(v == tag)) break;
  }
  asm volatile("" ::: "memory");
}
DI float red_g_max(float m) {
  auto r = __builtin_amdgcn_permlane16_swap(__float_as_uint(m), __float_as_uint(m), false, false);
  m = fmaxf(__uint_as_float(r[0]), __uint_as_float(r[1]));
  auto q = __builtin_amdgcn_permlane32_swap(__float_as_uint(m), __float_as_uint(m), false, false);
  return fmaxf(__uint_as_float(q[0]), __uint_as_float(q[1]));
}
DI float red_g_sum(float m) {
  auto r = __builtin_amdgcn_permlane16_swap(__float_as_uint(m), __float_as_uint(m), false, false);
  m = __uint_as_float(r[0]) + __uint_as_float(r[1]);
  auto q = __builtin_amdgcn_permlane32_swap(__float_as_uint(m), __float_as_uint(m), false, false);
  return __uint_as_float(q[0]) + __uint_as_float(q[1]);
}
DI unsigned slot_of(int u) { return (unsigned)((u + 4) % 9) * SL; }

__global__ __launch_bounds__(512, 2) void fused_block(const char* __restrict__ x, const char* __restrict__ wqkv,
                                                      const char* __restrict__ wo, const char* __restrict__ w1,
                                                      const char* __restrict__ b1, const char* __restrict__ w2,
                                                      const char* __restrict__ b2, char* __restrict__ out, char* ws) {
  __shared__ __attribute__((aligned(1024))) char smem[163840];
  asm volatile("" ::"v"(smem) : "memory");
  u64 ts[16];
#pragma unroll
  for (int q = 0; q < 16; ++q) ts[q] = 0;
  TS(0);
  const int lane = threadIdx.x & 63;
  const int wave = __builtin_amdgcn_readfirstlane(threadIdx.x >> 6);
  const int w = wave & 3;
  const int b = blockIdx.x;
  const int jh = b & 1, i = ((b >> 1) & 3) * 4 + (b >> 6), jj = (b >> 3) & 7;
  const int head = 8 * jh + jj, j = 7 * jh + jj;
  const bool isF = jj < 7;
  const int g = lane >> 4, n16 = lane & 15, q4 = (lane >> 2) & 3, pp = lane & 3;
  const f32x4 zero4 = {0.f, 0.f, 0.f, 0.f};
  const u32x4 ones_u = {0x3f803f80u, 0x3f803f80u, 0x3f803f80u, 0x3f803f80u};
  const __amdgpu_buffer_rsrc_t wsr = __builtin_amdgcn_make_buffer_rsrc(ws, 0, WS_BYTES, 0x00020000);

  if (wave >= 4) {
    const i32x4 xs = mksrd(x, 2048 * 896), w1s = mksrd(w1, 448 * 3584), w2s = mksrd(w2, 1792 * 896),
                wos = mksrd(wo, 448 * 896), wss = mksrd(ws, WS_BYTES);
    unsigned vx[4], vw[4];
#pragma unroll
    for (int t = 0; t < 4; ++t) {
      vx[t] = (unsigned)((lane >> 3) * 896 + (((lane & 7) ^ ((4 * t + g) & 7)) << 4) + t * (7168 - 1024));
      vw[t] = (unsigned)(g * 3584 + ((n16 ^ (2 * (g | ((t >> 1) << 2)))) << 4) + t * (4 * 3584 - 1024));
    }
    const unsigned sx = (unsigned)((128 * i + 32 * w) * 896), sw = (unsigned)(16 * w * 3584 + 256 * j);
    const unsigned mw = (unsigned)(w * 4096);
#define LOAD_U(u) do { if (((u) & 1) == 0) dma4(vx[0], vx[1], vx[2], vx[3], xs, sx + 128 * ((u) >> 1), slot_of(u) + mw); \
      else if (isF) dma4(vw[0], vw[1], vw[2], vw[3], w1s, sw + 64 * 3584 * ((u) >> 1), slot_of(u) + mw); } while (0)
#pragma unroll
    for (int u = 0; u < 9; ++u) LOAD_U(u);

    bf16x8 xf[2][14];
    float s1[2] = {0.f, 0.f}, s2[2] = {0.f, 0.f};
    const unsigned xa = (unsigned)((32 * w + n16) * 128), xsw = (unsigned)((n16 >> 1) & 7);
#pragma unroll
    for (int c = 0; c < 7; ++c) {
      if (c == 0) VMCNT(28); else if (c <= 3) VMCNT(20); else if (c == 4) VMCNT(16); else if (c == 5) VMCNT(8); else VMCNT(0);
      if (!isF) VMCNT(0);
      BARRIER();
      if (c == 0) TS(1);
      if (c == 1) { LOAD_U(9); LOAD_U(10); }
      if (c == 2) { LOAD_U(11); LOAD_U(12); }
      if (c == 3) { LOAD_U(13); }
#pragma unroll
      for (int rt = 0; rt < 2; ++rt)
#pragma unroll
        for (int ks = 0; ks < 2; ++ks) {
          bf16x8 f = lds_ld<bf16x8>(slot_of(2 * c) + xa + rt * 2048 + (((4 * ks + g) ^ xsw) << 4));
          xf[rt][2 * c + ks] = f;
          u32x4 fu = __builtin_bit_cast(u32x4, f);
#pragma unroll
          for (int e = 0; e < 4; ++e) {
            const unsigned fe = fu[e];
            const bf16x2 p = __builtin_bit_cast(bf16x2, fe);
            s1[rt] = __builtin_amdgcn_fdot2_f32_bf16(p, __builtin_bit_cast(bf16x2, 0x3f803f80u), s1[rt], false);
            s2[rt] = __builtin_amdgcn_fdot2_f32_bf16(p, p, s2[rt], false);
          }
        }
      asm volatile("" : "+v"(s1[0]), "+v"(s1[1]), "+v"(s2[0]), "+v"(s2[1]));
    }
    BARRIER();
    TS(2);
    const u64 tag = lds_ld<u64>(L_TAG);
    {
      const int s = lane / 3, p = lane - 3 * s;
      const bool act = lane < 42 && ((s & 3) == w || w == 2);
      gpoll(ws, WS_FRP + (unsigned)((p * 14 + s) * 4 + (head >> 2)) * FLAG_STRIDE, act, tag);
    }
    TS(3);
    const unsigned vl = (unsigned)(lane * 16);
    const unsigned wq0 = WS_WQ + (unsigned)head * WQ_HEAD;
#pragma unroll
    for (int t = 0; t < 4; ++t)
      if (t < 3 || w < 3) dma4c(vl, wss, wq0 + (unsigned)((4 * t + w) * 4096), (unsigned)((4 * t + w) * 4096));
#pragma unroll
    for (int t = 0; t < 4; ++t)
      if (t < 3 || w < 3) dma2c(vl, wss, wq0 + 61440 + (unsigned)((4 * t + w) * 2048), L_WQ + (unsigned)((4 * t + w) * 2048));
    unsigned v2[4];
#pragma unroll
    for (int t = 0; t < 4; ++t) v2[t] = (unsigned)((lane >> 3) * 896 + (((lane & 7) ^ (2 * g)) << 4) + t * (7168 - 1024));
    const unsigned s2b = (unsigned)((896 * jh + 32 * w) * 896 + 128 * jj);
#define LOAD_W2(n, slot) do { int sl_ = jj + (n); if (sl_ >= 7) sl_ -= 7; \
      dma4(v2[0], v2[1], v2[2], v2[3], w2s, s2b + (unsigned)(sl_ * 128 * 896), (slot) * SL + mw); } while (0)
    if (isF) { LOAD_W2(0, 6); LOAD_W2(1, 7); LOAD_W2(2, 8); }

    float rstd[2]; unsigned nm2[2];
#pragma unroll
    for (int rt = 0; rt < 2; ++rt) {
      const float mean = red_g_sum(s1[rt]) * (1.f / 448.f);
      const float var = fmaxf(red_g_sum(s2[rt]) * (1.f / 448.f) - mean * mean, 0.f);
      rstd[rt] = __builtin_amdgcn_rsqf(var + 1e-5f);
      nm2[rt] = pk2(-mean, -mean);
      if (g == 0) {
        lds_st<float>(L_RSTD + (unsigned)((32 * w + 16 * rt + n16) * 4), rstd[rt]);
        lds_st<float>(L_NMR + (unsigned)((32 * w + 16 * rt + n16) * 4), -mean * rstd[rt]);
      }
    }
    BARRIER();
    bf16x8 xaug[2];
#pragma unroll
    for (int rt = 0; rt < 2; ++rt) { const unsigned z = g == 3 ? 0u : nm2[rt]; xaug[rt] = asb(u32x4{nm2[rt], nm2[rt], z, z}); }
    f32x4 rsv[2];
#pragma unroll
    for (int rt = 0; rt < 2; ++rt) rsv[rt] = lds_ld<f32x4>(L_RSTD + (unsigned)((32 * w + 16 * rt + 4 * g) * 4));

    if (isF) VMCNT(18); else VMCNT(6);
    lsignal(C_A1, lane); lwait(C_A1);
    TS(4);
    f32x4 ak[2][2] = {{zero4, zero4}, {zero4, zero4}}, av[2][2] = {{zero4, zero4}, {zero4, zero4}};
#pragma unroll
    for (int s = 0; s < 15; ++s) {
      u32x4 wf[4];
#pragma unroll
      for (int t = 0; t < 4; ++t) {
        wf[t] = lds_ld<u32x4>(vl + (unsigned)(s * 4096 + t * 1024));
        if (s == 14 && g == 3) { wf[t][2] = 0u; wf[t][3] = 0u; }
      }
#pragma unroll
      for (int rt = 0; rt < 2; ++rt) {
        const bf16x8 xx = s < 14 ? xf[rt][s < 14 ? s : 0] : xaug[rt];
        ak[rt][0] = MFMA(asb(wf[0]), xx, ak[rt][0]);
        ak[rt][1] = MFMA(asb(wf[1]), xx, ak[rt][1]);
        av[rt][0] = MFMA(xx, asb(wf[2]), av[rt][0]);
        av[rt][1] = MFMA(xx, asb(wf[3]), av[rt][1]);
      }
    }
    VMCNT(0);
    lsignal(C_A2, lane);
    if (isF) lsignal(C_W2A, lane);
    u32x4 kvf[4];
    kvf[0] = pk8(ak[0][0] * rstd[0], ak[0][1] * rstd[0]);
    kvf[1] = pk8(ak[1][0] * rstd[1], ak[1][1] * rstd[1]);
    kvf[2] = pk8(av[0][0] * rsv[0], av[1][0] * rsv[1]);
    kvf[3] = pk8(av[0][1] * rsv[0], av[1][1] * rsv[1]);
    if (n16 == 12) kvf[3] = ones_u;
    const unsigned kvo = WS_KV + (unsigned)((i * 16 + head) * 16384 + w * 4096);
#pragma unroll
    for (int t = 0; t < 4; ++t) __builtin_amdgcn_raw_buffer_store_b128(kvf[t], wsr, vl + t * 1024, kvo, 16);
    lwait(C_A2);
#pragma unroll
    for (int t = 0; t < 4; ++t) lds_st<u32x4>(32768 + mw + vl + t * 1024, kvf[t]);
    TS(5);
    f32x4 aq[2][2] = {{zero4, zero4}, {zero4, zero4}};
#pragma unroll
    for (int s = 0; s < 15; ++s) {
      u32x4 wf[2];
#pragma unroll
      for (int t = 0; t < 2; ++t) {
        wf[t] = lds_ld<u32x4>(L_WQ + vl + (unsigned)(s * 2048 + t * 1024));
        if (s == 14 && g == 3) { wf[t][2] = 0u; wf[t][3] = 0u; }
      }
#pragma unroll
      for (int rt = 0; rt < 2; ++rt) {
        const bf16x8 xx = s < 14 ? xf[rt][s < 14 ? s : 0] : xaug[rt];
        aq[rt][0] = MFMA(asb(wf[0]), xx, aq[rt][0]);
        aq[rt][1] = MFMA(asb(wf[1]), xx, aq[rt][1]);
      }
    }
    VMCNT(0);
    gflag(ws, WS_FKV + (unsigned)((i * 16 + head) * 4 + w) * FLAG_STRIDE, tag, lane);
    lsignal(C_A3, lane);
    bf16x8 qf[2];
#pragma unroll
    for (int rt = 0; rt < 2; ++rt) {
      const float sc = rstd[rt] * (0.18898223650461362f * 1.4426950408889634f);
      u32x4 u = pk8(aq[rt][0] * sc, aq[rt][1] * sc);
      if (g == 3) u[2] = 0x00003f80u;
      qf[rt] = asb(u);
    }
    TS(6);
    if (isF) {
      lwait(C_A3); lwait(C_F1);
      LOAD_W2(3, 3); LOAD_W2(4, 4); LOAD_W2(5, 5); LOAD_W2(6, 6);
    }
    gpoll(ws, WS_FKV + (unsigned)(((i - 1 - (lane & 1)) * 16 + head) * 4 + w) * FLAG_STRIDE, lane < 2 && lane < i, tag);
    TS(7);
#pragma unroll
    for (int d = 1; d <= 2; ++d) {
      const unsigned dst = (unsigned)((2 - d) * 16384) + mw;
      if (i >= d) dma4c(vl, wss, kvo - (unsigned)(d * 16 * 16384), dst);
      else {
        const u32x4 kz = {0u, 0u, g == 3 ? 0x0000f149u : 0u, 0u}, vz = {0u, 0u, 0u, 0u};
        lds_st<u32x4>(dst + vl, kz); lds_st<u32x4>(dst + vl + 1024, kz);
        lds_st<u32x4>(dst + vl + 2048, vz); lds_st<u32x4>(dst + vl + 3072, vz);
      }
    }
    VMCNT(0);
    if (isF) lsignal(C_W2B, lane);
    lsignal(C_A4, lane); lwait(C_A4);
    TS(8);
    f32x4 cfirst, cdiag;
#pragma unroll
    for (int r = 0; r < 4; ++r) { cfirst[r] = (4 * g + r > n16) ? 0.f : -1e30f; cdiag[r] = (4 * g + r <= n16) ? 0.f : -1e30f; }
    const unsigned kb = mw + vl;
    f32x4 sa[18], sb[18];
#pragma unroll
    for (int t = 0; t < 18; ++t) {
      const bf16x8 kf = lds_ld<bf16x8>(kb + (unsigned)((t >> 1) * 4096 + (t & 1) * 1024));
      if (t < 17) sa[t] = MFMA(kf, qf[0], t == 0 ? cfirst : t == 16 ? cdiag : zero4);
      if (t > 0) sb[t] = MFMA(kf, qf[1], t == 1 ? cfirst : t == 17 ? cdiag : zero4);
    }
    float ma = sa[0][0], mb = sb[1][0];
#pragma unroll
    for (int t = 0; t < 17; ++t) {
      ma = fmaxf(fmaxf(ma, sa[t][0]), sa[t][1]); ma = fmaxf(fmaxf(ma, sa[t][2]), sa[t][3]);
      mb = fmaxf(fmaxf(mb, sb[t + 1][0]), sb[t + 1][1]); mb = fmaxf(fmaxf(mb, sb[t + 1][2]), sb[t + 1][3]);
    }
    ma = red_g_max(ma); mb = red_g_max(mb);
    f32x4 oa[2] = {zero4, zero4}, ob[2] = {zero4, zero4};
#pragma unroll
    for (int ks = 0; ks < 9; ++ks) {
      f32x4 pa[2], pb[2];
#pragma unroll
      for (int e = 0; e < 2; ++e) {
        const int t = 2 * ks + e;
#pragma unroll
        for (int r = 0; r < 4; ++r) {
          pa[e][r] = t < 17 ? __builtin_amdgcn_exp2f(sa[t][r] - ma) : 0.f;
          pb[e][r] = t > 0 ? __builtin_amdgcn_exp2f(sb[t][r] - mb) : 0.f;
        }
      }
      const bf16x8 pfa = asb(pk8(pa[0], pa[1])), pfb = asb(pk8(pb[0], pb[1]));
#pragma unroll
      for (int dt = 0; dt < 2; ++dt) {
        const bf16x8 vf = lds_ld<bf16x8>(kb + (unsigned)(ks * 4096 + 2048 + dt * 1024));
        oa[dt] = MFMA(vf, pfa, oa[dt]);
        ob[dt] = MFMA(vf, pfb, ob[dt]);
      }
    }
    lsignal(C_A5, lane);
    {
      const float la = __shfl(oa[1][0], 48 + n16), lb = __shfl(ob[1][0], 48 + n16);
      const float ra = __builtin_amdgcn_rcpf(la), rb = __builtin_amdgcn_rcpf(lb);
      u32x4 ua = pk8(oa[0] * ra, oa[1] * ra), ub = pk8(ob[0] * rb, ob[1] * rb);
      if (g == 3) { ua[2] = 0u; ua[3] = 0u; ub[2] = 0u; ub[3] = 0u; }
      const unsigned oo = WS_O + (unsigned)(((8 * i + 2 * w) * 16 + head) * 1024);
      __builtin_amdgcn_raw_buffer_store_b128(ua, wsr, vl, oo, 16);
      __builtin_amdgcn_raw_buffer_store_b128(ub, wsr, vl, oo + 16384, 16);
    }
    TS(9);
    VMCNT(0);
    gflag(ws, WS_FO + (unsigned)((i * 16 + head) * 4 + w) * FLAG_STRIDE, tag, lane);
    TS(10);
    if (isF) {
      lwait(C_A5); lwait(C_F2);
      const unsigned so = (unsigned)(112 * w * 896 + 128 * jj), mo = (unsigned)(w * 14336);
      dma4(v2[0], v2[1], v2[2], v2[3], wos, so, mo);
      dma4(v2[0], v2[1], v2[2], v2[3], wos, so + 32 * 896, mo + 4096);
      dma4(v2[0], v2[1], v2[2], v2[3], wos, so + 64 * 896, mo + 8192);
      dma2(v2[0], v2[1], wos, so + 96 * 896, mo + 12288);
      VMCNT(0);
      lsignal(C_WO, lane);
    }
    TS(11);
  } else if (!isF) {
    const i32x4 wqs = mksrd(wqkv, 448 * 2688);
    const int fw = ((i * 2 + jh) << 2) + w;
    const unsigned stg = slot_of(2 * w + 1);
    unsigned vr[7];
#pragma unroll
    for (int t = 0; t < 7; ++t) { const int c = 64 * t + lane, r = c / 14; vr[t] = (unsigned)(r * 2688 + (c - 14 * r) * 16 - (t & 3) * 1024); }
    const u64 e0 = __hip_atomic_load((GLBP(u64))(ws + WS_EPOCH), __ATOMIC_RELAXED, __HIP_MEMORY_SCOPE_AGENT);
    const int nun = fw < 40 ? 2 : 1;
    for (int r = 0; r < nun; ++r) {
      const int un = fw + 128 * r, po = un / 56, rem = un - 56 * po, s = rem >> 2, qd = rem & 3, p = po == 2 ? 0 : po + 1;
      const unsigned so = (unsigned)(32 * s * 2688 + p * 896 + qd * 224);
      dma4(vr[0], vr[1], vr[2], vr[3], wqs, so, stg + r * 7168);
      dma3(vr[4], vr[5], vr[6], wqs, so, stg + r * 7168 + 4096);
    }
    const u64 tag = e0 * 6364136223846793005ull + 1442695040888963407ull;
    if (wave == 0) {
      if (lane < 16) lds_st<unsigned>(L_CNT + 4 * lane, 0u);
      if (lane == 0) lds_st<u64>(L_TAG, tag);
    }
    VMCNT(0);
    TS(1);
    for (int r = 0; r < nun; ++r) {
      const int un = fw + 128 * r, po = un / 56, rem = un - 56 * po, s = rem >> 2, qd = rem & 3, p = po == 2 ? 0 : po + 1;
      const unsigned ra = stg + r * 7168 + (unsigned)((8 * g + q4) * 224 + pp * 8);
      const unsigned d0 = WS_WQ + (unsigned)(4 * qd) * WQ_HEAD + (p == 0 ? 61440u : (unsigned)((p - 1) * 2048));
      const unsigned kst = p == 0 ? 2048u : 4096u;
      float cs[2] = {0.f, 0.f};
#pragma unroll
      for (int hl = 0; hl < 4; ++hl)
#pragma unroll
        for (int tl = 0; tl < 2; ++tl) {
          u32x4 f = __builtin_bit_cast(u32x4, tr2(ra + hl * 56 + tl * 32, ra + hl * 56 + tl * 32 + 4 * 224));
          if (tl == 1 && n16 >= 12) f = u32x4{0u, 0u, 0u, 0u};
          __builtin_amdgcn_raw_buffer_store_b128(f, wsr, lane * 16 + tl * 1024, d0 + hl * WQ_HEAD + s * kst, 16);
          const f32x4 d = MFMA(asb(ones_u), asb(f), zero4);
          if (g == hl) cs[tl] = d[0];
        }
#pragma unroll
      for (int tl = 0; tl < 2; ++tl) {
        const unsigned hi = pk2(cs[tl], 0.f) & 0xffffu;
        const unsigned v = hi | (pk2(cs[tl] - bflo(hi), 0.f) << 16);
        __builtin_amdgcn_raw_buffer_store_b32(v, wsr, g * WQ_HEAD + n16 * 16 + tl * 1024,
                                              d0 + 14 * kst + (unsigned)((s >> 2) * 256 + (s & 3) * 4), 16);
      }
      VMCNT(0);
      gflag(ws, WS_FRP + (unsigned)((p * 14 + s) * 4 + qd) * FLAG_STRIDE, tag, lane);
    }
    TS(2);
    for (int c = 0; c < 9; ++c) BARRIER();
    if (b == 56 && w == 0) {
      for (int r = 0; r < 4; ++r) gpoll(ws, WS_FKV + (unsigned)((r * 64 + lane) * 4) * FLAG_STRIDE, true, tag);
      gflag(ws, WS_EPOCH, tag, lane);
    }
    TS(3);
  } else {
    if (wave == 0) {
      const u64 e0 = __hip_atomic_load((GLBP(u64))(ws + WS_EPOCH), __ATOMIC_RELAXED, __HIP_MEMORY_SCOPE_AGENT);
      if (lane < 16) lds_st<unsigned>(L_CNT + 4 * lane, 0u);
      if (lane == 0) lds_st<u64>(L_TAG, e0 * 6364136223846793005ull + 1442695040888963407ull);
    } else if (wave == 1) {
      const unsigned u = *(const unsigned*)(b1 + 256 * j + 4 * lane);
      lds_st<f32x2>(L_B1 + 8 * lane, f32x2{bflo(u), bfhi(u)});
    }
    u32x2 b2v[4];
#pragma unroll
    for (int ct = 0; ct < 4; ++ct) b2v[ct] = *(const u32x2*)(b2 + 128 * jj + 32 * ct + 8 * g);

    f32x4 acc[2][8], accs[2] = {zero4, zero4};
#pragma unroll
    for (int ct = 0; ct < 8; ++ct) { acc[0][ct] = zero4; acc[1][ct] = zero4; }
    u32x2 xres[4];
#pragma unroll
    for (int ct = 0; ct < 4; ++ct) xres[ct] = u32x2{0u, 0u};
    const unsigned xa = (unsigned)((16 * w + n16) * 128), xsw = (unsigned)((n16 >> 1) & 7);
    const unsigned wa = (unsigned)((8 * g + q4) * 256 + pp * 8 + (((q4 | ((g & 1) << 2)) ^ (2 * w)) << 5));
    bf16x8 xA[2], wA[8], xB[2], wB[8];
#define F_LOAD(X, W, xs_, ws_, ks) do { \
      X[0] = lds_ld<bf16x8>((xs_) + xa + (((4 * (ks) + g) ^ xsw) << 4)); \
      X[1] = lds_ld<bf16x8>((xs_) + xa + 8192 + (((4 * (ks) + g) ^ xsw) << 4)); \
      _Pragma("unroll") for (int t = 0; t < 8; ++t) { const unsigned a_ = (ws_) + (wa ^ (unsigned)(t << 5)) + (ks) * 8192; W[t] = tr2(a_, a_ + 1024); } } while (0)
#define F_MMA(X, W) do { \
      _Pragma("unroll") for (int t = 0; t < 8; ++t) { acc[0][t] = MFMA(W[t], X[0], acc[0][t]); acc[1][t] = MFMA(W[t], X[1], acc[1][t]); } \
      accs[0] = MFMA(W[0], asb(ones_u), accs[0]); accs[1] = MFMA(W[1], asb(ones_u), accs[1]); } while (0)
    unsigned xsl = 4 * SL, wsl = 5 * SL;
    BARRIER();
    TS(1);
    F_LOAD(xA, wA, xsl, wsl, 0);
#pragma unroll 1
    for (int c = 0; c < 7; ++c) {
      F_LOAD(xB, wB, xsl, wsl, 1);
      if (c == jj) {
#pragma unroll
        for (int ct = 0; ct < 4; ++ct)
          xres[ct] = lds_ld<u32x2>(xsl + xa + (unsigned)(jh * 8192) + (((2 * ct + (g >> 1)) ^ xsw) << 4) + (g & 1) * 8);
      }
      SCHED();
      F_MMA(xA, wA);
      SCHED();
      BARRIER();
      xsl += 2 * SL; if (xsl >= 9 * SL) xsl -= 9 * SL;
      wsl += 2 * SL; if (wsl >= 9 * SL) wsl -= 9 * SL;
      if (c < 6) F_LOAD(xA, wA, xsl, wsl, 0);
      SCHED();
      F_MMA(xB, wB);
      SCHED();
    }
    if (n16 == 0) {
      lds_st<f32x4>(L_CS + (unsigned)((32 * w + 4 * g) * 4), accs[0]);
      lds_st<f32x4>(L_CS + (unsigned)((32 * w + 16 + 4 * g) * 4), accs[1]);
    }
    BARRIER();
    TS(2);
    const u64 tag = lds_ld<u64>(L_TAG);
    u32x4 hf[2][4];
    const unsigned vl = (unsigned)(lane * 16);
    {
      float rs[2], nm[2];
#pragma unroll
      for (int rt = 0; rt < 2; ++rt) {
        rs[rt] = lds_ld<float>(L_RSTD + (unsigned)((64 * rt + 16 * w + n16) * 4));
        nm[rt] = lds_ld<float>(L_NMR + (unsigned)((64 * rt + 16 * w + n16) * 4));
      }
#pragma unroll
      for (int kk = 0; kk < 4; ++kk) {
        f32x4 hv[2][2];
#pragma unroll
        for (int e = 0; e < 2; ++e) {
          const int t = 2 * kk + e;
          const unsigned co = (unsigned)(((t ^ (2 * w)) * 16 + 4 * g) * 4);
          const f32x4 cs = lds_ld<f32x4>(L_CS + co), bb = lds_ld<f32x4>(L_B1 + co);
#pragma unroll
          for (int rt = 0; rt < 2; ++rt) {
            const f32x4 v = acc[rt][t] * rs[rt] + (cs * nm[rt] + bb);
            const f32x4 z = v * (v * v * (-0.10294324f) + (-2.3022082f));
            f32x4 r;
#pragma unroll
            for (int q = 0; q < 4; ++q) r[q] = __builtin_amdgcn_rcpf(1.f + __builtin_amdgcn_exp2f(z[q]));
            hv[rt][e] = v * r;
          }
        }
#pragma unroll
        for (int rt = 0; rt < 2; ++rt) {
          hf[rt][kk] = pk8(hv[rt][0], hv[rt][1]);
          __builtin_amdgcn_raw_buffer_store_b128(hf[rt][kk], wsr, vl,
              WS_H + (unsigned)(((8 * i + 4 * rt + w) * 56 + 4 * j + (kk ^ w)) * 1024), 16);
        }
      }
    }
    TS(3);
    f32x4 oc[2][4];
#pragma unroll
    for (int ct = 0; ct < 4; ++ct) { oc[0][ct] = zero4; oc[1][ct] = zero4; }
    const unsigned w2a = (unsigned)((4 * g + q4) * 128 + pp * 8 + (((2 * g + (q4 >> 1)) & 3) << 5));
    VMCNT(0);
    gflag(ws, WS_FH + (unsigned)((i * 16 + j) * 4 + w) * FLAG_STRIDE, tag, lane);
    TS(14);
    lwait(C_W2A);
#pragma unroll
    for (int kk = 0; kk < 4; ++kk) {
      const unsigned base = 6 * SL + (unsigned)((kk ^ w) * 4096);
#pragma unroll
      for (int ct = 0; ct < 4; ++ct) {
        const unsigned a = base + (w2a ^ (unsigned)(ct << 5));
        const bf16x8 wf = tr2(a, a + 2048);
        oc[0][ct] = MFMA(wf, asb(hf[0][kk]), oc[0][ct]);
        oc[1][ct] = MFMA(wf, asb(hf[1][kk]), oc[1][ct]);
      }
    }
    lsignal(C_F1, lane);
    TS(4);
    gpoll(ws, WS_FH + (unsigned)((i * 16 + 7 * jh + (lane & 7)) * 4 + w) * FLAG_STRIDE, lane < 7 && lane != jj, tag);
    TS(5);
    const unsigned hb = WS_H + (unsigned)(((8 * i + w) * 56 + 28 * jh) * 1024);
    u32x4 hr[3][2][4];
#define H_LOAD(n) do { int sl_ = jj + (n); if (sl_ >= 7) sl_ -= 7; \
      _Pragma("unroll") for (int kk = 0; kk < 4; ++kk) _Pragma("unroll") for (int rt = 0; rt < 2; ++rt) \
        hr[((n) - 1) % 3][rt][kk] = __builtin_amdgcn_raw_buffer_load_b128(wsr, vl + kk * 1024, hb + (unsigned)(rt * 4 * 56 * 1024 + sl_ * 4096), 16); } while (0)
    H_LOAD(1); H_LOAD(2); H_LOAD(3);
#pragma unroll
    for (int n = 1; n < 7; ++n) {
      if (n == 3) lwait(C_W2B);
      const unsigned sbase = (unsigned)(n < 3 ? 6 + n : n) * SL;
#pragma unroll
      for (int kk = 0; kk < 4; ++kk) {
#pragma unroll
        for (int ct = 0; ct < 4; ++ct) {
          const unsigned a = sbase + kk * 4096 + (w2a ^ (unsigned)(ct << 5));
          const bf16x8 wf = tr2(a, a + 2048);
          oc[0][ct] = MFMA(wf, asb(hr[(n - 1) % 3][0][kk]), oc[0][ct]);
          oc[1][ct] = MFMA(wf, asb(hr[(n - 1) % 3][1][kk]), oc[1][ct]);
        }
      }
      if (n == 3) lsignal(C_F2, lane);
      if (n + 3 < 7) H_LOAD(n + 3);
    }
    TS(6);
    f32x4 own[4];
#pragma unroll
    for (int ct = 0; ct < 4; ++ct) {
      const f32x4 snd = jh ? oc[0][ct] : oc[1][ct];
      own[ct] = jh ? oc[1][ct] : oc[0][ct];
      __builtin_amdgcn_raw_buffer_store_b128(__builtin_bit_cast(u32x4, snd), wsr, vl + ct * 1024,
                                             WS_P + (unsigned)(((i * 16 + j) * 4 + w) * 4096), 16);
    }
    VMCNT(0);
    gflag(ws, WS_FP + (unsigned)((i * 16 + j) * 4 + w) * FLAG_STRIDE, tag, lane);
    TS(7);
    gpoll(ws, WS_FO + (unsigned)((i * 16 + n16) * 4 + ((4 * jh + w) >> 1)) * FLAG_STRIDE, lane < 16, tag);
    TS(8);
    u32x4 of[16];
#pragma unroll
    for (int h = 0; h < 16; ++h)
      of[h] = __builtin_amdgcn_raw_buffer_load_b128(wsr, vl + (h & 3) * 1024,
                                                    WS_O + (unsigned)(((8 * i + 4 * jh + w) * 16 + (h & ~3)) * 1024), 16);
    lwait(C_WO);
    TS(9);
#pragma unroll
    for (int h = 0; h < 16; ++h) {
#pragma unroll
      for (int ct = 0; ct < 4; ++ct) {
        const unsigned a = (unsigned)(h * 28 * 128) + (w2a ^ (unsigned)((ct ^ (2 * (h & 1))) << 5));
        own[ct] = MFMA(tr2(a, a + 2048), asb(of[h]), own[ct]);
      }
    }
    TS(10);
    const int jp = j + 7 - 14 * jh;
    gpoll(ws, WS_FP + (unsigned)((i * 16 + jp) * 4 + w) * FLAG_STRIDE, lane == 0, tag);
    TS(11);
    char* orow = out + (size_t)(128 * i + 64 * jh + 16 * w + n16) * 896 + 128 * jj + 8 * g;
#pragma unroll
    for (int ct = 0; ct < 4; ++ct) {
      const f32x4 pv = __builtin_bit_cast(f32x4, __builtin_amdgcn_raw_buffer_load_b128(
          wsr, vl + ct * 1024, WS_P + (unsigned)(((i * 16 + jp) * 4 + w) * 4096), 16));
      const f32x4 xr = {bflo(xres[ct][0]), bfhi(xres[ct][0]), bflo(xres[ct][1]), bfhi(xres[ct][1])};
      const f32x4 bb = {bflo(b2v[ct][0]), bfhi(b2v[ct][0]), bflo(b2v[ct][1]), bfhi(b2v[ct][1])};
      const f32x4 r = own[ct] + pv + xr + bb;
      *(u32x2*)(orow + 32 * ct) = u32x2{pk2(r[0], r[1]), pk2(r[2], r[3])};
    }
    TS(12);
  }
  VMCNT(0);
  TS(13);
  if (lane == 0) {
    u64* o = (u64*)(ws + WS_DIAG) + (size_t)(b * 8 + wave) * 16;
#pragma unroll
    for (int q = 0; q < 16; ++q) o[q] = ts[q];
  }
}

__global__ void k_pre(u64* ts){ if(threadIdx.x==0) ts[40000+blockIdx.x]=__builtin_amdgcn_s_memrealtime(); }
__global__ void k_delay(const u64* ts,int mode){
  u64 tstart=__builtin_amdgcn_s_memrealtime();
  __shared__ u64 smn[256], smx[256], spre[256];
  int b=threadIdx.x; u64 mn=~0ull,mx=0;
  for(int w=0;w<8;w++){ u64 s=ts[(b*8+w)*16]; mn=s<mn?s:mn; u64 e=ts[(b*8+w)*16+13]; mx=e>mx?e:mx; }
  smn[b]=mn; smx[b]=mx; spre[b]=ts[40000+b]; __syncthreads();
  if(b==0){ u64 pre=0; for(int q=0;q<256;q++){ mn=smn[q]<mn?smn[q]:mn; mx=smx[q]>mx?smx[q]:mx; pre=spre[q]>pre?spre[q]:pre; }
    u64 v= mode==0? tstart-mx : mode==1 ? mn-pre : mode==3 ? mx-mn : 0; if(v>5000) v=5000;
    u64 t0=__builtin_amdgcn_s_memrealtime();
    while(__builtin_amdgcn_s_memrealtime()-t0 < v*100) __builtin_amdgcn_s_sleep(8); }
}

extern "C" void kernel_launch(void* const* d_in, const int* in_sizes, int n_in, void* d_out, int out_size, void* d_ws,
                              size_t ws_size, hipStream_t stream) {
  k_pre<<<256,64,0,stream>>>((u64*)((char*)d_ws+WS_DIAG));
  fused_block<<<256, 512, 0, stream>>>((const char*)d_in[0], (const char*)d_in[1], (const char*)d_in[2], (const char*)d_in[4],
                                       (const char*)d_in[5], (const char*)d_in[6], (const char*)d_in[7], (char*)d_out,
                                       (char*)d_ws);
}
```

```cpp
#include <hip/hip_runtime.h>
#include <hip/hip_bf16.h>

typedef __bf16 bf16x2 __attribute__((ext_vector_type(2)));
typedef __bf16 bf16x4 __attribute__((ext_vector_type(4)));
typedef __bf16 bf16x8 __attribute__((ext_vector_type(8)));
typedef float f32x2 __attribute__((ext_vector_type(2)));
typedef float f32x4 __attribute__((ext_vector_type(4)));
typedef float f32x16 __attribute__((ext_vector_type(16)));
typedef unsigned u32x2 __attribute__((ext_vector_type(2)));
typedef unsigned u32x4 __attribute__((ext_vector_type(4)));
typedef int i32x4 __attribute__((ext_vector_type(4)));
typedef unsigned long long u64;
#define LDSP(T) __attribute__((address_space(3))) T*
#define DI __device__ __forceinline__
#define MFMA16(a, b, c) __builtin_amdgcn_mfma_f32_16x16x32_bf16((a), (b), (c), 0, 0, 0)
#define MMA(acc, a, b) asm volatile("v_mfma_f32_32x32x16_bf16 %0, %1, %2, %0" : "+v"(acc) : "v"(a), "v"(b))
#define MMA0(acc, a, b) asm volatile("v_mfma_f32_32x32x16_bf16 %0, %1, %2, 0" : "=&v"(acc) : "v"(a), "v"(b))

constexpr unsigned WS_EPOCH = 0;
constexpr unsigned WS_FRP = 4096;
constexpr unsigned WS_FL = 21u << 20;
constexpr unsigned FL_K = 0, FL_V = 128, FL_O = 256, FL_H = 384, FL_P = 512;
constexpr unsigned WS_CS = 864u << 10;
constexpr unsigned WS_ST = 896u << 10;
constexpr unsigned WS_WQ = 1u << 20;
constexpr unsigned WQ_HEAD = 96 * 1024, WQ_PART = 32 * 1024;
constexpr unsigned WS_H = 3u << 20;
constexpr unsigned WS_KV = 11u << 20;
constexpr unsigned WS_O = 15u << 20;
constexpr unsigned WS_P = 17u << 20;
constexpr unsigned WS_DIAG = 22u << 20;
constexpr unsigned WS_BYTES = 24u << 20;

constexpr unsigned SL = 16384;
constexpr unsigned L_MISC = 9 * SL;
constexpr unsigned L_CNT = L_MISC;
constexpr unsigned L_RSTD = L_MISC + 128;
constexpr unsigned L_XAUG = L_MISC + 1024;
constexpr unsigned L_B1 = L_MISC + 3072;
constexpr unsigned L_JUNK = L_MISC + 3584;
constexpr unsigned L_WAUG = L_MISC + 4096;
constexpr unsigned L_STG = L_MISC + 8192;
constexpr unsigned L_WK = 2 * SL, L_WV = 4 * SL, L_WQ = 0;
constexpr unsigned L_K = 2 * SL, L_V = 24576;
constexpr unsigned L_XCH = 2 * SL;
enum { C_A0 = 0, C_U, C_A1, C_A2, C_A3, C_A4, C_A5, C_A6, C_A7, C_A8, C_F1, C_F2, C_F3, C_F4, C_WO, C_X, C_RP, C_F5, C_F6 };
constexpr unsigned OOB = 0x80000000u;

#define P_PEEK 1
#define P_CHECK 12
#define PEEK_AT -1
constexpr int SPIN_G = 20000;
constexpr int SPIN_L = 400000;
constexpr unsigned CODE_BYTES = 26880;

#define SCHED() __builtin_amdgcn_sched_barrier(0)
#define TS(n) do { } while (0)
#define PINV(v) asm volatile("" : "+v"(v))
#define PINS(v) asm volatile("" : "+s"(v))
#define FENCE() asm volatile("" ::: "memory")
#define VMCNT(n) asm volatile("s_waitcnt vmcnt(" #n ")" ::: "memory")
#define LGKM0() do { FENCE(); __builtin_amdgcn_s_waitcnt(0xc07f); FENCE(); } while (0)
#define BARRIER() do { LGKM0(); asm volatile("s_barrier" ::: "memory"); } while (0)

template <int V> struct IC { static constexpr int value = V; };
template <int B, int E, class F> DI void static_for(F&& f) { if constexpr (B < E) { f(IC<B>{}); static_for<B + 1, E>(f); } }
template <class T> DI T lds_ld(unsigned a) { return *(LDSP(T))(size_t)a; }
template <class T> DI void lds_st(unsigned a, T v) { *(LDSP(T))(size_t)a = v; }
DI bf16x4 tr1(unsigned a) { return __builtin_amdgcn_ds_read_tr16_b64_v4bf16((LDSP(bf16x4))(size_t)a); }
DI bf16x8 cat(bf16x4 lo, bf16x4 hi) { return __builtin_shufflevector(lo, hi, 0, 1, 2, 3, 4, 5, 6, 7); }
DI unsigned pk2(float a, float b) { f32x2 v = {a, b}; return __builtin_bit_cast(unsigned, __builtin_convertvector(v, bf16x2)); }
DI bf16x8 asb(u32x4 v) { return __builtin_bit_cast(bf16x8, v); }
DI float bflo(unsigned u) { return __uint_as_float(u << 16); }
DI float bfhi(unsigned u) { return __uint_as_float(u & 0xffff0000u); }
DI unsigned split2(float v) { const unsigned hi = pk2(v, v); return pk2(v, v - bflo(hi)); }
DI float fmul(float a, float b) { float r; asm("v_mul_f32 %0, %1, %2" : "=v"(r) : "v"(a), "v"(b)); return r; }
DI float fsub(float a, float b) { float r; asm("v_sub_f32 %0, %1, %2" : "=v"(r) : "v"(a), "v"(b)); return r; }
DI float ffma(float a, float b, float c) { float r; asm("v_fma_f32 %0, %1, %2, %3" : "=v"(r) : "v"(a), "v"(b), "v"(c)); return r; }

DI i32x4 mksrd(const void* p, unsigned bytes) {
  i32x4 r; r[0] = (int)(size_t)p; r[1] = (int)(((size_t)p) >> 32) & 0xffff; r[2] = (int)bytes; r[3] = 0x00020000; return r;
}
#define SETM0 "s_add_u32 m0, %[mb], %[mc]\n\ts_nop 0\n\t"
template <unsigned M> DI void dma4(const unsigned (&v)[4], i32x4 rs, unsigned so, unsigned mb) {
  asm volatile(SETM0 "buffer_load_dwordx4 %0, %[rs], %[so] offen lds\n\tbuffer_load_dwordx4 %1, %[rs], %[so] offen offset:1024 lds\n\t"
               "buffer_load_dwordx4 %2, %[rs], %[so] offen offset:2048 lds\n\tbuffer_load_dwordx4 %3, %[rs], %[so] offen offset:3072 lds"
               ::"v"(v[0]), "v"(v[1]), "v"(v[2]), "v"(v[3]), [mb] "s"(mb), [mc] "n"(M), [rs] "s"(rs), [so] "s"(so) : "memory", "scc");
}
template <unsigned M> DI void dma2(unsigned v0, unsigned v1, i32x4 rs, unsigned so, unsigned mb) {
  asm volatile(SETM0 "buffer_load_dwordx4 %0, %[rs], %[so] offen lds\n\tbuffer_load_dwordx4 %1, %[rs], %[so] offen offset:1024 lds"
               ::"v"(v0), "v"(v1), [mb] "s"(mb), [mc] "n"(M), [rs] "s"(rs), [so] "s"(so) : "memory", "scc");
}
template <unsigned M> DI void dma_dw(unsigned v, i32x4 rs, unsigned so, unsigned mb) {
  asm volatile(SETM0 "buffer_load_dword %0, %[rs], %[so] offen lds"
               ::"v"(v), [mb] "s"(mb), [mc] "n"(M), [rs] "s"(rs), [so] "s"(so) : "memory", "scc");
}
template <int T, unsigned M> DI void dma1(unsigned v, i32x4 rs, unsigned so, unsigned mb) {
  if constexpr (T == 0) asm volatile(SETM0 "buffer_load_dwordx4 %0, %[rs], %[so] offen lds"
                                     ::"v"(v), [mb] "s"(mb), [mc] "n"(M), [rs] "s"(rs), [so] "s"(so) : "memory", "scc");
  else asm volatile("buffer_load_dwordx4 %0, %1, %2 offen offset:%3 lds" ::"v"(v), "s"(rs), "s"(so), "n"(T * 1024) : "memory");
}
template <unsigned M> DI void dma4c(unsigned v, i32x4 rs, unsigned so, unsigned mb) {
  asm volatile(SETM0 "buffer_load_dwordx4 %0, %[rs], %[so] offen sc1 lds\n\tbuffer_load_dwordx4 %0, %[rs], %[so] offen offset:1024 sc1 lds\n\t"
               "buffer_load_dwordx4 %0, %[rs], %[so] offen offset:2048 sc1 lds\n\tbuffer_load_dwordx4 %0, %[rs], %[so] offen offset:3072 sc1 lds"
               ::"v"(v), [mb] "s"(mb), [mc] "n"(M), [rs] "s"(rs), [so] "s"(so) : "memory", "scc");
}
template <unsigned M> DI void dma2c(unsigned v, i32x4 rs, unsigned so, unsigned mb) {
  asm volatile(SETM0 "buffer_load_dwordx4 %0, %[rs], %[so] offen sc1 lds\n\tbuffer_load_dwordx4 %0, %[rs], %[so] offen offset:1024 sc1 lds"
               ::"v"(v), [mb] "s"(mb), [mc] "n"(M), [rs] "s"(rs), [so] "s"(so) : "memory", "scc");
}
template <int T, unsigned M> DI void dma1c(unsigned v, i32x4 rs, unsigned so, unsigned mb) {
  if constexpr (T == 0) asm volatile(SETM0 "buffer_load_dwordx4 %0, %[rs], %[so] offen sc1 lds"
                                     ::"v"(v), [mb] "s"(mb), [mc] "n"(M), [rs] "s"(rs), [so] "s"(so) : "memory", "scc");
  else asm volatile("buffer_load_dwordx4 %0, %1, %2 offen offset:%3 sc1 lds" ::"v"(v), "s"(rs), "s"(so), "n"(T * 1024) : "memory");
}
#define PEEK_ISSUE(dst, off, imm) asm volatile("buffer_load_dwordx2 %0, %1, %2, 0 offen offset:%3 sc1" : "=v"(dst) : "v"(off), "s"(wss), "n"(imm) : "memory")
#define PEEK_WAIT(dst, n) asm volatile("s_waitcnt vmcnt(%1)" : "+v"(dst) : "n"(n) : "memory")
DI void st16(u32x4 v, __amdgpu_buffer_rsrc_t r, unsigned off) { __builtin_amdgcn_raw_buffer_store_b128(v, r, off, 0, 16); FENCE(); }
DI void st8(u32x2 v, __amdgpu_buffer_rsrc_t r, unsigned off) { __builtin_amdgcn_raw_buffer_store_b64(v, r, off, 0, 16); FENCE(); }
DI u32x4 ld16(__amdgpu_buffer_rsrc_t r, unsigned voff, unsigned soff) { return __builtin_amdgcn_raw_buffer_load_b128(r, voff, soff, 16); }

DI void lmark(int c, unsigned cm, unsigned one) {
  FENCE(); *(volatile LDSP(unsigned char))(size_t)(cm + 4 * c) = (unsigned char)one; FENCE();
}
DI unsigned lpeek(int c, unsigned cb) { return *(volatile LDSP(unsigned))(size_t)(cb + 4 * c); }
DI void lwait(int c, unsigned cb, unsigned mask = 0x01010101u) {
#pragma unroll 1
  for (int s = 0; s < SPIN_L; ++s)
    if ((__builtin_amdgcn_readfirstlane(lpeek(c, cb)) & mask) == mask) break;
  FENCE();
}
DI void lcheck(int c, unsigned cb, unsigned peeked) {
  if (__builtin_expect(__builtin_amdgcn_readfirstlane(peeked) != 0x01010101u, 0)) lwait(c, cb);
  FENCE();
}
DI void gpoll(__amdgpu_buffer_rsrc_t r, unsigned off, u64 need, u64 tag) {
#pragma unroll 1
  for (int s = 0; s < SPIN_G; ++s) {
    FENCE();
    const u64 v = __builtin_bit_cast(u64, __builtin_amdgcn_raw_buffer_load_b64(r, off, 0, 16));
    if ((__ballot(v == tag) & need) == need) break;
    __builtin_amdgcn_s_sleep(2);
  }
  FENCE();
}
template <int C> DI float dpp_add(float v) {
  return v + __uint_as_float(__builtin_amdgcn_update_dpp(0u, __float_as_uint(v), C, 0xf, 0xf, true));
}
DI float sum32(float v) {
  v = dpp_add<0xb1>(v); v = dpp_add<0x4e>(v); v = dpp_add<0x141>(v); v = dpp_add<0x140>(v);
  auto q = __builtin_amdgcn_permlane16_swap(__float_as_uint(v), __float_as_uint(v), false, false);
  const unsigned a = q[0], b = q[1];
  return __uint_as_float(a) + __uint_as_float(b);
}
DI f32x2 halves(float m) {
  auto q = __builtin_amdgcn_permlane32_swap(__float_as_uint(m), __float_as_uint(m), false, false);
  const unsigned a = q[0], b = q[1];
  return f32x2{__uint_as_float(a), __uint_as_float(b)};
}
template <int S> DI u32x4 packs(const f32x16& a, float sc) {
  return u32x4{pk2(fmul(a[8 * S], sc), fmul(a[8 * S + 1], sc)), pk2(fmul(a[8 * S + 2], sc), fmul(a[8 * S + 3], sc)),
               pk2(fmul(a[8 * S + 4], sc), fmul(a[8 * S + 5], sc)), pk2(fmul(a[8 * S + 6], sc), fmul(a[8 * S + 7], sc))};
}

__global__ __launch_bounds__(512, 2) void fused_block(const char* __restrict__ x, const char* __restrict__ wqkv,
                                                      const char* __restrict__ wo, const char* __restrict__ w1,
                                                      const char* __restrict__ b1, const char* __restrict__ w2,
                                                      const char* __restrict__ b2, char* __restrict__ out, char* ws) {
  __shared__ __attribute__((aligned(1024))) char smem[163840];
  const int wave = __builtin_amdgcn_readfirstlane(threadIdx.x >> 6);
  unsigned j0, j1, j2, j3;
  {
    u64 pc0;
    const unsigned ln = (blockIdx.x >> 3) * 4 + wave - 4;
    asm volatile("s_cmp_ge_u32 %6, %7\n\ts_cbranch_scc1 .Lnowarm%=\n\ts_cmp_lt_u32 %8, 4\n\ts_cbranch_scc1 .Lnowarm%=\n\t"
                 "s_getpc_b64 %0\n\ts_load_dword %1, %0, %5\n\ts_load_dword %2, %0, %5 offset:64\n\t"
                 "s_load_dword %3, %0, %5 offset:128\n\ts_load_dword %4, %0, %5 offset:192\n.Lnowarm%=:"
                 : "=&s"(pc0), "=&s"(j0), "=&s"(j1), "=&s"(j2), "=&s"(j3) : "s"(ln * 256), "s"(ln), "n"(CODE_BYTES / 256), "s"(wave) : "scc");
  }
  const int lane = threadIdx.x & 63;
  const int w = wave & 3;
  const int b = blockIdx.x;
  const int jh = b & 1, i = ((b >> 1) & 3) * 4 + (b >> 6), jj = (b >> 3) & 7;
  const int head = 8 * jh + jj, j = 7 * jh + jj;
  const bool isF = jj < 7;
  const int g = lane >> 4, n16 = lane & 15, q4 = (lane >> 2) & 3, pp = lane & 3, r32 = lane & 31, hh = lane >> 5;
#define MOD_(v, m) ((v) >= (m) ? (v) - (m) : (v))
  const int kh = i & 1;
  const int rot = isF ? ((jj + i) & 1) * 3 : 4 * kh;
#define CH(t) MOD_(rot + (t), 7)
#define SLICES(sl) int sl[7]; sl[0] = isF ? jj : 0; _Pragma("unroll") for (int n_ = 1; n_ < 7; ++n_) sl[n_] = MOD_(sl[n_ - 1] + (i & 3) + 1, 7)
#define W2_SO(s_) ((unsigned)((896 * jh + 32 * w) * 896 + 128 * jj) + (unsigned)(s_) * (128u * 896u))
  const int jw = isF ? j : 7 * jh + (i >> 1);
  unsigned mw = (unsigned)(w * 4096);
  unsigned vx[4], vw[4];
#pragma unroll
  for (int t = 0; t < 4; ++t) {
    vx[t] = (unsigned)((lane >> 3) * 896 + (((lane & 7) ^ ((4 * t + g) & 7)) << 4) + t * (7168 - 1024));
    vw[t] = (unsigned)(g * 3584 + ((n16 ^ (4 * g)) << 4) + t * (4 * 3584 - 1024));
    PINV(vx[t]); PINV(vw[t]);
  }
  unsigned xo = (unsigned)((128 * i + 32 * w) * 896 + 128 * rot), wo_ = (unsigned)((16 * w + 64 * rot) * 3584 + 256 * jw);
  int left = 6 - rot;
  PINS(mw); PINS(xo); PINS(wo_); PINS(left);
  asm volatile("s_waitcnt lgkmcnt(0)" : "+s"(j0), "+s"(j1), "+s"(j2), "+s"(j3)
               : "s"(x), "s"(wqkv), "s"(wo), "s"(w1), "s"(b1), "s"(w2), "s"(b2), "s"(out), "s"(ws), "v"((LDSP(char))smem) : "memory");
  const i32x4 xs = mksrd(x, 2048 * 896);
  i32x4 w1s = mksrd(w1, (isF || i < 14) ? 448 * 3584 : 0);
#define LOAD_X(c) do { dma4<(2 * (c)) % 6 * SL>(vx, xs, xo, mw); xo += left == (c) ? (unsigned)(-6 * 128) : 128u; PINS(xo); } while (0)
#define LOAD_W(c) do { dma4<(2 * (c) + 1) % 6 * SL>(vw, w1s, wo_, mw); \
    wo_ += left == (c) ? (unsigned)(-6 * 64 * 3584) : (unsigned)(64 * 3584); PINS(wo_); } while (0)
  if (wave < 4) {
    LOAD_X(0); LOAD_W(0); LOAD_X(1); LOAD_W(1);
  }
  u64 tag = *(const __attribute__((address_space(4))) u64*)(size_t)(ws + WS_EPOCH);
  const f32x4 zero4 = {0.f, 0.f, 0.f, 0.f};
  f32x16 zero16;
#pragma unroll
  for (int e = 0; e < 16; ++e) zero16[e] = 0.f;
  const __amdgpu_buffer_rsrc_t wsr = __builtin_amdgcn_make_buffer_rsrc(ws, 0, WS_BYTES, 0x00020000);
  const i32x4 wss = mksrd(ws, WS_BYTES);
  const unsigned fme = WS_FL + (unsigned)((i * 16 + head) * 4 + w) * 1024;
#define COMMON_SETUP() \
  unsigned vl = (unsigned)(lane * 16), cb = L_CNT, cm = L_CNT + w, one = 1; \
  PINV(vl); PINV(cb); PINV(cm); PINV(one)
#define V2_SETUP() \
  unsigned vfl = lane < 16 ? fme + lane * 8 : OOB, v2[4]; PINV(vfl); \
  _Pragma("unroll") for (int t = 0; t < 4; ++t) { \
    v2[t] = (unsigned)((lane >> 3) * 896 + (((lane & 7) ^ (4 * (g & 1))) << 4) + t * (7168 - 1024)); PINV(v2[t]); }
#define TAG_SETUP() \
  tag = tag * 6364136223846793005ull + 1442695040888963407ull; PINS(tag); \
  const unsigned tagl = (unsigned)tag, tagh = (unsigned)(tag >> 32) & 0xffu; \
  u32x2 tagv = __builtin_bit_cast(u32x2, tag); PINV(tagv)
#define GRANULE(v) u32x2{tagl, ((__float_as_uint(v) + 0x80u) & 0xffffff00u) | tagh}
#define GOOD(v) ((v)[0] == tagl && ((v)[1] & 0xffu) == tagh)
#define SEEN(v) __ballot(__builtin_bit_cast(u64, v) == tag)
#define FLAG(kind) st8(tagv, wsr, vfl + (kind))
#define MARK_(c) lmark(c, cm, one)

  int role = wave; PINS(role);
  if (__builtin_expect(role < 4, 1)) {
    const bool rpk = b < 168;
    const int rpo = b >= 112 ? 2 : b >= 56 ? 1 : 0, rc = (b - 56 * rpo) >> 3, rpr = b & 7, rp = rpo == 2 ? 0 : rpo + 1;
    {
      const int t0 = w < 3 ? 2 * w : 5;
      unsigned vr[2];
#pragma unroll
      for (int t = 0; t < 2; ++t) { const int ci = 64 * (t0 + t) + lane, r = (ci * 9363) >> 16; vr[t] = (unsigned)(r * (2688 - 112) + ci * 16 - t * 1024); }
      dma2<L_STG>(vr[0], vr[1], mksrd(wqkv, rpk ? 448 * 2688 : 0), (unsigned)(64 * rc * 2688 + rp * 896 + rpr * 112), (unsigned)t0 * 1024);
      dma_dw<L_B1>((unsigned)(lane * 4), (w & 1) ? mksrd(b2 + 128 * jj, isF ? 128 : 0) : mksrd(b1 + 256 * j, isF ? 256 : 0), 0u, (unsigned)(w & 1) * 256);
    }
    TS(1);
    COMMON_SETUP();
    TAG_SETUP();
    if (wave == 0 && lane < 36) lds_st<unsigned>(lane < 32 ? L_CNT + 4 * lane : L_STG + 7168 - 128 + 4 * lane, 0u);
    unsigned xa[4], xb[4];
#pragma unroll
    for (int s = 0; s < 4; ++s) {
      xa[s] = (unsigned)((32 * w + r32) * 128 + (((2 * s + hh) ^ ((r32 >> 1) & 7)) << 4)); xb[s] = xa[s] + 4 * SL; PINV(xa[s]); PINV(xb[s]);
    }
    unsigned rowo = (unsigned)((128 * i + 8 * head + 2 * w) * 896), ro0 = (unsigned)(hh * 896 + r32 * 16), ro1 = r32 < 24 ? ro0 : OOB;
    PINS(rowo); PINV(ro0); PINV(ro1);
    u32x4 xf[28], r0, r1;
#define X_READ(c) do { _Pragma("unroll") for (int s = 0; s < 4; ++s) \
      xf[4 * (c) + s] = lds_ld<u32x4>((c) % 3 == 2 ? xb[s] : xa[s] + ((c) % 3) * 2 * SL); SCHED(); } while (0)
    VMCNT(11);
    BARRIER();
    TS(16);
    asm volatile("buffer_load_dwordx4 %0, %2, %4, %5 offen\n\tbuffer_load_dwordx4 %1, %3, %4, %5 offen offset:512"
                 : "=&v"(r0), "=&v"(r1) : "v"(ro0), "v"(ro1), "s"(xs), "s"(rowo) : "memory");
    X_READ(0); LOAD_X(2); LOAD_W(2);
    unsigned ra = L_STG + (unsigned)((8 * hh + q4) * 112 + (g & 1) * 32 + pp * 8 + (w & 1) * 56);
    unsigned rb = ra + (unsigned)((w & 2) ? 0 : 32 * 112);
    ra += (unsigned)((w & 2) * 16 * 112);
    const unsigned d0 = rpk ? WS_WQ + (unsigned)(2 * rpr + (w & 1)) * WQ_HEAD + (unsigned)rpo * WQ_PART : OOB;
    unsigned rst = d0 + (unsigned)((4 * rc + (w & 2)) * 1024) + vl;
    unsigned rsa = (hh || w >= 2) ? OOB : d0 + 28 * 1024 + (unsigned)(rc * 128 + r32 * 4);
    unsigned rfl = (rpk && w == 0 && lane < 16) ? WS_FRP + (unsigned)(((rp * 7 + rc) * 8 + rpr) * 128 + lane * 8) : OOB;
    unsigned sel1 = ((n16 ^ g) & 1) ? 0u : 0x3f803f80u, keep = r32 < 28 ? ~0u : 0u;
    unsigned gso = (r32 >> 1) == 7 ? WS_ST + (unsigned)((128 * i + 8 * head + 2 * w + hh) * 16 + (r32 & 1) * 8) : OOB;
    unsigned zso = lane < 40 ? fme + vl : OOB;
    unsigned tc2 = lane < 13 ? (unsigned)((((b >> 1) * 4 + w) * 13 + lane) * 128) : OOB, tc3 = lane < 4 ? (unsigned)(((b * 4 + w) * 4 + lane) * 128) : OOB;
    i32x4 t2s = mksrd(w2 + 896 * 896 * jh, 896 * 896), t3s = mksrd(wo, 448 * 896);
    PINV(ra); PINV(rb); PINV(rst); PINV(rsa); PINV(rfl); PINV(sel1); PINV(keep); PINV(gso); PINV(zso); PINV(tc2); PINV(tc3); PINS(t2s); PINS(t3s);
    SCHED();
    VMCNT(10);
    BARRIER();
    TS(17);
    X_READ(1);
    {
      u32x4 f[4];
#pragma unroll
      for (int s = 0; s < 4; ++s) {
        const unsigned a = (s < 2 ? ra : rb) + (s & 1) * 16 * 112;
        f[s] = __builtin_bit_cast(u32x4, cat(tr1(a), tr1(a + 4 * 112)));
      }
      SCHED();
      LOAD_X(3);
      const u32x4 sel = {sel1, sel1, sel1, sel1};
      f32x4 cs = MFMA16(asb(sel), asb(f[0]), zero4), cs2 = MFMA16(asb(sel), asb(f[1]), zero4);
      cs = MFMA16(asb(sel), asb(f[2]), cs); cs2 = MFMA16(asb(sel), asb(f[3]), cs2);
      f[0] &= keep; f[1] &= keep;
      PINV(f[0]); PINV(f[1]); PINV(cs); PINV(cs2);
      SCHED();
      LOAD_W(3);
      st16(f[0], wsr, rst); st16(f[1], wsr, rst + 1024);
      __builtin_amdgcn_raw_buffer_store_b32(split2((g & 1) ? cs[1] + cs2[1] : cs[0] + cs2[0]) & keep, wsr, rsa, 0, 16);
      FENCE();
      st16(u32x4{~tagl, 0u, ~tagl, 0u}, wsr, zso);
      dma_dw<L_JUNK>(tc2, t2s, 0u, 0u); dma_dw<L_JUNK>(tc3, t3s, 0u, 0u);
    }
    asm volatile("s_waitcnt vmcnt(14)" : "+v"(r0), "+v"(r1)::"memory");
    BARRIER();
    TS(18);
    X_READ(2); LOAD_X(4);
    {
      float s1a = 0.f, s1b = 0.f, s2a = 0.f, s2b = 0.f;
#define ST6(n) "v_lshlrev_b32 %0, 16, %" #n "\n\tv_and_b32 %1, %10, %" #n "\n\tv_add_f32 %2, %2, %0\n\tv_add_f32 %3, %3, %1\n\tv_fmac_f32 %4, %0, %0\n\tv_fmac_f32 %5, %1, %1\n\t"
#define ST24(rr) do { float lo, hi; asm volatile(ST6(6) ST6(7) ST6(8) ST6(9) \
                       : "=&v"(lo), "=&v"(hi), "+v"(s1a), "+v"(s1b), "+v"(s2a), "+v"(s2b) \
                       : "v"(rr[0]), "v"(rr[1]), "v"(rr[2]), "v"(rr[3]), "s"(0xffff0000u)); } while (0)
      ST24(r0); ST24(r1);
      if (!isF) w1s[2] = 0;
      LOAD_W(4);
      const float a = s1a + s1b, q = s2a + s2b;
      const float a2 = dpp_add<0xb1>(a), q2 = dpp_add<0xb1>(q);
      float v = (lane & 1) ? q2 : a2;
      v = dpp_add<0x4e>(v); v = dpp_add<0x114>(v); v = dpp_add<0x118>(v);
      auto sw = __builtin_amdgcn_permlane16_swap(__float_as_uint(v), __float_as_uint(v), false, false);
      const unsigned s0 = sw[0], s1 = sw[1];
      v = (__uint_as_float(s0) + __uint_as_float(s1)) * (1.f / 448.f);
      const float mean = __uint_as_float(__builtin_amdgcn_update_dpp(0u, __float_as_uint(v), 0xa0, 0xf, 0xf, true));
      const float rs = __builtin_amdgcn_rsqf(fmaxf(v - mean * mean, 0.f) + 1e-5f);
      st8(GRANULE((lane & 1) ? rs : v), wsr, gso);
    }
    VMCNT(12);
    BARRIER();
    TS(19);
    st8(tagv, wsr, rfl);
    X_READ(3); LOAD_X(5);
    V2_SETUP();
    unsigned mw2 = (unsigned)w * 2048, xro = (unsigned)((128 * i + 64 * jh + 16 * w) * 896 + 128 * (isF ? jj : 0));
    unsigned rfo = lane < 21 ? WS_FRP + (unsigned)(lane * 8 + (head >> 1)) * 128 : OOB;
    unsigned sto = WS_ST + (unsigned)((128 * i + 32 * w + r32) * 16 + hh * 8);
    unsigned cso = WS_CS + (unsigned)((jw * 4 + w) * 512 + lane * 8);
    PINS(mw2); PINS(xro); PINV(rfo); PINV(sto); PINV(cso);
    SCHED();
    LOAD_W(5);
    dma2<L_STG>(vx[0], vx[1], xs, xro, mw2);
    VMCNT(12);
    BARRIER();
    TS(20);
    X_READ(4); LOAD_X(6);
    i32x4 w2s = mksrd(w2, isF ? 1792 * 896 : 0);
    unsigned w2o[4];
    SLICES(sl);
#pragma unroll
    for (int n = 1; n < 3; ++n) { w2o[n] = W2_SO(sl[n]); PINS(w2o[n]); }
    PINS(w2s);
    SCHED();
    LOAD_W(6);
    u32x2 prp, pst, pcs;
    PEEK_ISSUE(prp, rfo, 0); PEEK_ISSUE(pst, sto, 0); PEEK_ISSUE(pcs, cso, 0);
    VMCNT(13);
    BARRIER();
    TS(21);
    X_READ(5);
    dma4<7 * SL>(v2, w2s, w2o[1], mw);
    unsigned wqa = WS_WQ + (unsigned)head * WQ_HEAD + (unsigned)CH(w) * 4096u;
    unsigned wqb = WS_WQ + (unsigned)head * WQ_HEAD + (unsigned)(w < 3 ? CH(w + 4) : 7) * 4096u;
    unsigned vlb = w < 3 ? vl : OOB, vla = (w < 3 || lane < 56) ? vl : OOB, aga = (unsigned)(28 * 1024 + hh * 512 + r32 * 4), agk = aga + L_WK;
    PINV(vla); PINV(aga); PINV(agk);
    unsigned lxa = hh ? L_RSTD + (unsigned)((32 * w + r32) * 4) : L_XAUG + (unsigned)((32 * w + r32) * 16);
    unsigned lwa = L_WAUG + (unsigned)(w * 1024) + vl;
    unsigned lrs = L_RSTD + (unsigned)((32 * w + 4 * hh) * 4);
    unsigned b1u = lds_ld<unsigned short>(L_B1 + (unsigned)(64 * w + 2 * r32));
    b1u = hh ? 0u : b1u;
    unsigned kvs = WS_KV + (unsigned)((i * 16 + head) * 16384 + w * 2048);
    unsigned kst = i < 15 ? kvs + vl : OOB, vst = kst + 8192, kb = L_K + mw2 + vl, ka = L_WK + vl;
    unsigned fnb = (lane < 2 && lane < i) ? fme - (unsigned)(lane + 1) * 64 * 1024 : OOB;
    unsigned vn1 = i >= 1 ? vl : OOB, vn2 = i >= 2 ? vl : OOB;
    unsigned nbo1 = kvs - (i >= 1 ? 16 * 16384 : 0), nbo2 = kvs - (i >= 2 ? 2 * 16 * 16384 : 0);
    unsigned ost = WS_O + (unsigned)(((4 * i + w) * 16 + head) * 2048) + vl;
    u64 nbneed = i >= 2 ? 3ull : (u64)i;
    PINS(wqa); PINS(wqb); PINV(vlb); PINV(lxa); PINV(lwa); PINV(lrs); PINV(b1u);
    PINV(kst); PINV(vst); PINV(kb); PINV(ka); PINV(fnb); PINV(vn1); PINV(vn2); PINS(nbo1); PINS(nbo2); PINV(ost); PINS(nbneed);
#define WQ_B(T, M, p) dma1c<T, M>((T) == 0 ? vla : vlb, wss, wqb + (p) * WQ_PART, mw)
#define WQ_B4(M, p) do { WQ_B(0, M, p); WQ_B(1, M, p); WQ_B(2, M, p); WQ_B(3, M, p); } while (0)
#define NB_LOAD(part) do { dma2c<L_K + (part) * L_V>(vn2, wss, nbo2 + (part) * 8192, mw2); \
                           dma2c<L_K + (part) * L_V + 8192>(vn1, wss, nbo1 + (part) * 8192, mw2); } while (0)
    SCHED();
    dma4<8 * SL>(v2, w2s, w2o[2], mw);
    VMCNT(11);
    BARRIER();
    TS(22);
    X_READ(6);
    asm volatile("s_waitcnt vmcnt(8)" : "+v"(prp), "+v"(pst), "+v"(pcs)::"memory");
    const bool early = (SEEN(prp) & 0x1fffffull) == 0x1fffffull;
    if (early) dma4c<L_WK>(vl, wss, wqa, mw);
    if (__builtin_expect(!__all(GOOD(pst) && (GOOD(pcs) || !isF)), 0)) {
#pragma unroll 1
      for (int s = 0; s < SPIN_G; ++s) {
        pst = __builtin_amdgcn_raw_buffer_load_b64(wsr, sto, 0, 16);
        pcs = __builtin_amdgcn_raw_buffer_load_b64(wsr, cso, 0, 16);
        FENCE();
        if (__all(GOOD(pst) && (GOOD(pcs) || !isF))) break;
      }
    }
    float rstd; unsigned nm2;
    {
      const f32x2 mr = halves(__uint_as_float(pst[1] & 0xffffff00u));
      rstd = mr[1];
      const unsigned m = split2(-mr[0]), sd = split2(__builtin_amdgcn_rcpf(rstd));
      nm2 = pk2(-mr[0], -mr[0]);
      if (hh == 0) lds_st<u32x4>(lxa, u32x4{nm2, (m >> 16) | (sd << 16), sd >> 16, __float_as_uint(rstd)});
      else lds_st<float>(lxa, rstd);
      const unsigned cs = split2(__uint_as_float(pcs[1] & 0xffffff00u));
      lds_st<u32x4>(lwa, u32x4{cs, (cs & 0xffffu) | (b1u << 16), b1u, 0u});
    }
    TS(3);
    BARRIER();
    TS(23);
    if (__builtin_expect(!early, 0)) { gpoll(wsr, rfo, 0x1fffffull, tag); dma4c<L_WK>(vl, wss, wqa, mw); }
    MARK_(C_RP);
    WQ_B4(L_WK + SL, 0);
    dma4c<L_WV>(vl, wss, wqa + WQ_PART, mw);
    VMCNT(8);
    MARK_(C_A0);
    WQ_B4(L_WV + SL, 1);
    u32x4 xaug = {nm2, nm2, nm2, hh ? 0u : nm2};
    float one28 = r32 == 28 ? 1.f : 0.f;
    float qsc = rstd * (0.18898223650461362f * 1.4426950408889634f);
    unsigned augm = hh ? 0u : ~0u;
    int mrel = r32 - 4 * hh;
    float big = -1e30f, zf = 0.f; PINV(big); PINV(zf);
    PINV(xaug); PINV(one28); PINV(qsc); PINV(augm); PINV(mrel);
    TS(25);
    lwait(C_A0, cb);
    TS(4);
    f32x16 ak, av, aq, sc_[9];
    u32x2 pk, pk2_, pv_;
    bool kok;
    {
      u32x4 wf[7], kf2[2], vf2[2];
      f32x4 rsv[4];
      unsigned pe;
#define AUG_A(n) ((n) < 29 ? agk : (n) < 58 ? agk + 2 * SL : aga)
#define QKV_READ(n) do { if ((n) % 29 == 28) wf[(n) % 7] = u32x4{lds_ld<unsigned>(AUG_A(n)), lds_ld<unsigned>(AUG_A(n) + 128), lds_ld<unsigned>(AUG_A(n) + 256), lds_ld<unsigned>(AUG_A(n) + 384)}; \
        else wf[(n) % 7] = lds_ld<u32x4>((n) < 29 ? ka + (unsigned)((n) * 1024) : (n) < 58 ? ka + (unsigned)(((n) + 3) * 1024) : vl + (unsigned)(((n) - 58) * 1024)); } while (0)
#pragma unroll
      for (int n = 0; n < 6; ++n) QKV_READ(n);
      SCHED();
      static_for<0, 87>([&](auto N) __attribute__((always_inline)) {
        constexpr int n = decltype(N)::value, p = n / 29, s = n % 29;
        if constexpr (n == 4) { VMCNT(8); MARK_(C_U); }
        if constexpr (n == 9) lcheck(C_U, cb, pe);
        if constexpr (n == 17) { VMCNT(0); MARK_(C_A1); }
        if constexpr (n == 22) lcheck(C_A1, cb, pe);
        if constexpr (n == 29) { TS(26); MARK_(C_A2); }
        if constexpr (n == 41) lcheck(C_A2, cb, pe);
        if constexpr (n == 51) lcheck(C_A3, cb, pe);
        if constexpr (n == 54) { VMCNT(0); FLAG(FL_K); }
        if constexpr (n == 58) { TS(28); MARK_(C_A4); }
        if constexpr (n == 70) lcheck(C_A4, cb, pe);
        if constexpr (n == 81) {
          PEEK_WAIT(pk, 2);
          kok = (SEEN(pk) & nbneed) == nbneed;
          if (kok) NB_LOAD(0);
          PEEK_ISSUE(pk2_, fnb, FL_K);
        }
        if constexpr (n == 85) { if (kok) VMCNT(5); else VMCNT(1); FLAG(FL_V); }
        const u32x4 xx = s < 28 ? xf[s < 28 ? s : 0] : xaug;
        if constexpr (p == 0) { if constexpr (s == 0) MMA0(ak, wf[n % 7], xx); else MMA(ak, wf[n % 7], xx); }
        if constexpr (p == 1) { if constexpr (s == 0) MMA0(av, xx, wf[n % 7]); else MMA(av, xx, wf[n % 7]); }
        if constexpr (p == 2) { if constexpr (s == 0) MMA0(aq, wf[n % 7], xx); else MMA(aq, wf[n % 7], xx); }
        if constexpr (n + 6 < 87) QKV_READ(n + 6);
        if constexpr (n == 5) pe = lpeek(C_U, cb);
        if constexpr (n == 18) pe = lpeek(C_A1, cb);
        if constexpr (n == 36) pe = lpeek(C_A2, cb);
        if constexpr (n == 46) pe = lpeek(C_A3, cb);
        if constexpr (n == 65) pe = lpeek(C_A4, cb);
        if constexpr (n >= 53 && n < 57) rsv[n - 53] = lds_ld<f32x4>(lrs + 32 * (n - 53));
        if constexpr (n == 65) PEEK_ISSUE(pk, fnb, FL_K);
        if constexpr (n >= 32 && n < 40) {
          constexpr int d = n - 32;
          unsigned u; float t0;
          asm volatile("v_mul_f32 %0, %2, %4\n\tv_mul_f32 %1, %3, %4\n\tv_cvt_pk_bf16_f32 %0, %0, %1"
                       : "=&v"(u), "=&v"(t0) : "v"(ak[2 * d]), "v"(ak[2 * d + 1]), "v"(rstd));
          kf2[d >> 2][d & 3] = u;
          if constexpr ((d & 3) == 3) st16(kf2[d >> 2], wsr, kst + (d >> 2) * 1024);
        }
        if constexpr (n == 41) { lds_st<u32x4>(kb + 16384, kf2[0]); lds_st<u32x4>(kb + 16384 + 1024, kf2[1]); }
        if constexpr (n >= 61 && n < 69) {
          constexpr int d = n - 61;
          unsigned u; float t0;
          asm volatile("v_fma_f32 %0, %2, %4, %6\n\tv_fma_f32 %1, %3, %5, %6\n\tv_cvt_pk_bf16_f32 %0, %0, %1"
                       : "=&v"(u), "=&v"(t0) : "v"(av[2 * d]), "v"(av[2 * d + 1]), "v"(rsv[d >> 1][2 * (d & 1)]), "v"(rsv[d >> 1][2 * (d & 1) + 1]), "v"(one28));
          vf2[d >> 2][d & 3] = u;
          if constexpr (d == 5) st16(vf2[0], wsr, vst);
          if constexpr (d == 7) st16(vf2[1], wsr, vst + 1024);
        }
        if constexpr (n == 70) { lds_st<u32x4>(kb + L_V + 16384, vf2[0]); lds_st<u32x4>(kb + L_V + 16384 + 1024, vf2[1]); }
        if constexpr (n == 31) asm volatile("s_nop 4" : "+v"(ak));
        if constexpr (n == 60) asm volatile("s_nop 4" : "+v"(av));
        if constexpr (n >= 71) {
          constexpr int e = n - 71;
          float m0, m8;
          asm volatile("v_cmp_gt_i32 vcc, %3, %2\n\ts_nop 1\n\tv_cndmask_b32 %0, %4, %5, vcc\n\tv_cndmask_b32 %1, %5, %4, vcc"
                       : "=&v"(m0), "=&v"(m8) : "v"(mrel), "n"((e & 3) + 8 * (e >> 2)), "v"(big), "v"(zf) : "vcc");
          sc_[0][e] = m0; sc_[8][e] = m8;
        }
        SCHED();
      });
    }
    MARK_(C_A5);
    TS(5);
    asm volatile("s_nop 10" : "+v"(aq));
    u32x4 qf[2];
    qf[0] = packs<0>(aq, qsc); qf[1] = packs<1>(aq, qsc);
    if (hh) qf[1][2] = 0x00003f80u;
    PINV(qf[0]); PINV(qf[1]); PINV(sc_[0]); PINV(sc_[8]);
    TS(6);
    PEEK_WAIT(pk2_, 1);
    if (__builtin_expect(!kok, 0)) {
      if ((SEEN(pk2_) & nbneed) != nbneed) gpoll(wsr, fnb + FL_K, nbneed, tag);
      NB_LOAD(0);
      VMCNT(0);
    }
    if (__builtin_expect(i < 2, 0)) {
      if (hh) {
        lds_st<unsigned>(kb + 1024 + 8, 0x0000f149u);
        if (i < 1) lds_st<unsigned>(kb + 8192 + 1024 + 8, 0x0000f149u);
      }
    }
    TS(7);
    LGKM0();
    MARK_(C_A6);
    PEEK_ISSUE(pv_, fnb, FL_V);
    lwait(C_A6, cb);
    TS(8);
    float mx = -1e30f, mx2 = -1e30f;
    {
      bf16x8 kf[4][2];
#pragma unroll
      for (int t = 0; t < 3; ++t) { kf[t][0] = lds_ld<bf16x8>(kb + t * 2048); kf[t][1] = lds_ld<bf16x8>(kb + t * 2048 + 1024); }
      SCHED();
      static_for<0, 11>([&](auto T) __attribute__((always_inline)) {
        constexpr int t = decltype(T)::value;
        if constexpr (t < 9) { if constexpr (t == 0 || t == 8) MMA(sc_[t], kf[t & 3][0], qf[0]); else MMA0(sc_[t], kf[t & 3][0], qf[0]); }
        if constexpr (t + 3 < 9) kf[(t + 3) & 3][0] = lds_ld<bf16x8>(kb + (t + 3) * 2048);
        if constexpr (t == 9) {
          PEEK_WAIT(pv_, 0);
          if (__builtin_expect((SEEN(pv_) & nbneed) != nbneed, 0)) gpoll(wsr, fnb + FL_V, nbneed, tag);
          NB_LOAD(1);
        }
        if constexpr (t == 2 || t == 10) asm volatile("s_nop 7" : "+v"(sc_[t - 2]));
        if constexpr (t >= 2) {
#pragma unroll
          for (int e = 0; e < 8; e += 4) {
            mx = fmaxf(fmaxf(mx, sc_[t - 2][e]), sc_[t - 2][e + 1]); mx2 = fmaxf(fmaxf(mx2, sc_[t - 2][e + 2]), sc_[t - 2][e + 3]);
          }
          PINV(mx); PINV(mx2);
        }
        SCHED();
        if constexpr (t < 9) MMA(sc_[t], kf[t & 3][1], qf[1]);
        if constexpr (t + 3 < 9) kf[(t + 3) & 3][1] = lds_ld<bf16x8>(kb + (t + 3) * 2048 + 1024);
        if constexpr (t >= 2) {
#pragma unroll
          for (int e = 8; e < 16; e += 4) {
            mx = fmaxf(fmaxf(mx, sc_[t - 2][e]), sc_[t - 2][e + 1]); mx2 = fmaxf(fmaxf(mx2, sc_[t - 2][e + 2]), sc_[t - 2][e + 3]);
          }
          PINV(mx); PINV(mx2);
        }
        SCHED();
      });
    }
    TS(29);
    u32x4 mneg, mone = {0x3f803f80u & augm, 0x00003f80u & augm, 0u, 0u};
    {
      const f32x2 hm = halves(fmaxf(mx, mx2));
      const float m = -fmaxf(hm[0], hm[1]);
      const unsigned h12 = split2(m);
      mneg = u32x4{h12, pk2(m - bflo(h12) - bfhi(h12), 0.f), 0u, 0u};
      asm volatile("s_nop 1" : "+v"(mneg), "+v"(mone));
      MMA(sc_[0], mone, mneg); MMA(sc_[1], mone, mneg);
      asm volatile("s_nop 10" : "+v"(sc_[0]));
    }
    f32x16 oa;
    {
      u32x4 pf[18];
      bf16x8 vf[6];
      unsigned pe;
#define PV_DONE(u) ((u) <= 11 ? 0 : 3 * ((u) - 11) < (u) - 1 ? 3 * ((u) - 11) : (u) - 1)
#define V_READ(q) vf[(q) % 6] = lds_ld<bf16x8>(kb + L_V + (unsigned)((q) * 1024))
#define PV(q) do { if constexpr ((q) == 0) MMA0(oa, vf[0], pf[0]); else MMA(oa, vf[(q) % 6], pf[q]); \
                   if constexpr ((q) + 6 < 18) V_READ((q) + 6); SCHED(); } while (0)
      static_for<0, 18>([&](auto U) __attribute__((always_inline)) {
        constexpr int u = decltype(U)::value, np = PV_DONE(u + 1) - PV_DONE(u);
        if constexpr (u == 8) { VMCNT(0); MARK_(C_A7); }
        if constexpr (u == 9) pe = lpeek(C_A7, cb);
        if constexpr (u == 10) { lcheck(C_A7, cb, pe); TS(12); V_READ(0); V_READ(1); V_READ(2); V_READ(3); V_READ(4); V_READ(5); }
        float z[8];
#define S_(e) sc_[u >> 1][8 * (u & 1) + (e)]
        if constexpr (np == 3) PV(PV_DONE(u));
        if constexpr (u + 2 < 9) { MMA(sc_[u + 2], mone, mneg); SCHED(); }
        asm volatile("v_exp_f32 %0, %4\n\tv_exp_f32 %1, %5\n\tv_exp_f32 %2, %6\n\tv_exp_f32 %3, %7"
                     : "=&v"(z[0]), "=&v"(z[1]), "=&v"(z[2]), "=&v"(z[3]) : "v"(S_(0)), "v"(S_(1)), "v"(S_(2)), "v"(S_(3)));
        SCHED();
        if constexpr (np == 1) PV(PV_DONE(u));
        if constexpr (np == 3) PV(PV_DONE(u) + 1);
        asm volatile("v_exp_f32 %0, %4\n\tv_exp_f32 %1, %5\n\tv_exp_f32 %2, %6\n\tv_exp_f32 %3, %7"
                     : "=&v"(z[4]), "=&v"(z[5]), "=&v"(z[6]), "=&v"(z[7]) : "v"(S_(4)), "v"(S_(5)), "v"(S_(6)), "v"(S_(7)));
        SCHED();
        if constexpr (np == 3) PV(PV_DONE(u) + 2);
        unsigned p0, p1, p2, p3;
        asm volatile("v_cvt_pk_bf16_f32 %0, %4, %5\n\tv_cvt_pk_bf16_f32 %1, %6, %7\n\tv_cvt_pk_bf16_f32 %2, %8, %9\n\tv_cvt_pk_bf16_f32 %3, %10, %11"
                     : "=&v"(p0), "=&v"(p1), "=&v"(p2), "=&v"(p3)
                     : "v"(z[0]), "v"(z[1]), "v"(z[2]), "v"(z[3]), "v"(z[4]), "v"(z[5]), "v"(z[6]), "v"(z[7]));
        pf[u] = u32x4{p0, p1, p2, p3};
        PINV(pf[u]);
        SCHED();
      });
      asm volatile("s_nop 1");
      MMA(oa, vf[17 % 6], pf[17]);
    }
    MARK_(C_A8);
    TS(30);
    asm volatile("s_nop 11" : "+v"(oa));
    {
      float rl = __builtin_amdgcn_rcpf(halves(oa[12])[1]);
      asm volatile("s_nop 0" : "+v"(rl));
      const u32x4 u0 = packs<0>(oa, rl);
      st16(u0, wsr, ost);
      u32x4 u1 = packs<1>(oa, rl);
      u1[2] &= augm; u1[3] &= augm;
      st16(u1, wsr, ost + 1024);
    }
    TS(9);
    VMCNT(0);
    FLAG(FL_O);
    TS(11);
    if (__builtin_expect(b == 248 && w == 0, 0)) {
#pragma unroll 1
      for (int r = 0; r < 4; ++r) gpoll(wsr, WS_FL + FL_K + (unsigned)((r * 64 + lane) * 4) * 1024, ~0ull, tag);
      st8(tagv, wsr, lane == 0 ? WS_EPOCH : OOB);
    }
  } else {
    COMMON_SETUP();
    V2_SETUP();
    const bool keeper = (w >> 1) == jh;
    const int fct = keeper ? 0 : 1;
    const int frt = 2 * jh + (w & 1);
    f32x16 acc[4] = {zero16, zero16, zero16, zero16};
    const unsigned xsw = (unsigned)((r32 >> 1) & 7);
    unsigned xa[4], wa[4];
#pragma unroll
    for (int t = 0; t < 4; ++t) {
      xa[t] = (unsigned)((32 * w + r32) * 128) + (((2 * t + hh) ^ xsw) << 4); PINV(xa[t]);
      wa[t] = SL + (unsigned)((8 * hh + q4) * 256 + (g & 1) * 32 + pp * 8 + ((q4 ^ t) << 6)); PINV(wa[t]);
      PINV(acc[t]);
    }
    i32x4 w2s = mksrd(w2, 1792 * 896);
    unsigned w20 = W2_SO(0), w2own = W2_SO(jj);
    unsigned wqa = WS_WQ + (unsigned)head * WQ_HEAD + 2 * WQ_PART + (unsigned)CH(w) * 4096u;
    unsigned wqb = WS_WQ + (unsigned)head * WQ_HEAD + 2 * WQ_PART + (unsigned)(w < 3 ? CH(w + 4) : 7) * 4096u;
    unsigned vlb = w < 3 ? vl : OOB, vla = (w < 3 || lane < 56) ? vl : OOB;
    PINV(vla);
#define WQ_LOAD() do { dma4c<L_WQ>(vl, wss, wqa, mw); WQ_B(0, L_WQ + SL, 0); WQ_B(1, L_WQ + SL, 0); WQ_B(2, L_WQ + SL, 0); WQ_B(3, L_WQ + SL, 0); } while (0)
    unsigned lxg = L_XAUG + (unsigned)((32 * w + r32) * 16), wga = L_WAUG + vl;
    unsigned hso = WS_H + (unsigned)(((4 * i + w) * 112 + 8 * j) * 1024) + vl, hso4 = hso + 4096;
    unsigned slp = 0;
    {
      SLICES(sl);
#pragma unroll
      for (int n = 0; n < 7; ++n) slp |= (unsigned)sl[n] << (4 * n);
    }
    PINS(w2s); PINS(w20); PINS(w2own); PINS(wqa); PINS(wqb); PINV(vlb); PINV(lxg); PINV(wga); PINV(hso); PINV(hso4); PINS(slp);
#define W2O(n) (w20 + ((slp >> (4 * (n))) & 7u) * (128u * 896u))
    TAG_SETUP();
    if (__builtin_expect(!isF, 0)) {
    {
      unsigned wc = SL + (unsigned)((8 * hh + q4) * 256 + (g & 1) * 32 + pp * 8 + ((q4 ^ w) << 6));
      const unsigned so1 = ((n16 ^ g) & 1) ? 0u : 0x3f803f80u;
      const u32x4 sel = {so1, so1, so1, so1};
      f32x4 cs = zero4;
#pragma unroll 1
      for (int c = 0; c < 4 - kh; ++c) {
        BARRIER();
#pragma unroll
        for (int s = 0; s < 4; ++s) cs = MFMA16(asb(sel), cat(tr1(wc + s * 4096), tr1(wc + s * 4096 + 1024)), cs);
        wc += c == 2 ? (unsigned)(-4 * (int)SL) : 2 * SL;
      }
      st8(GRANULE((g & 1) ? cs[1] : cs[0]), wsr, (hh || i >= 14) ? OOB : WS_CS + (unsigned)((jw * 4 + w) * 512 + kh * 256 + r32 * 8));
    }
#pragma unroll 1
    for (int q = 0; q < 4 + kh; ++q) BARRIER();
    lwait(C_RP, cb);
    WQ_LOAD();
    VMCNT(0);
    MARK_(C_A3);
    } else {
    bf16x8 xA[2], xB[2];
    bf16x4 wA[8][2], wB[8][2];
#define F_READ(X, W, ks, r) do { if ((r) == 0 || (r) == 9) X[(r) / 9] = lds_ld<bf16x8>(xa[2 * (ks) + (r) / 9]); \
      else { const int s_ = (r) / 9, q_ = (r) % 9 - 1; \
        W[4 * s_ + (q_ >> 1)][q_ & 1] = tr1(wa[q_ >> 1] + (2 * (ks) + s_) * 4096 + (q_ & 1) * 1024); } } while (0)
#define F_MMA(X, W, m) do { MMA(acc[(m) & 3], cat(W[m][0], W[m][1]), X[(m) >> 2]); SCHED(); } while (0)
#define F_RD(XN, WN, ksn, a, n) do { _Pragma("unroll") for (int r_ = (a); r_ < (a) + (n); ++r_) F_READ(XN, WN, ksn, r_); SCHED(); } while (0)
#define F_HEAD(X, W, XN, WN, ksn) do { F_MMA(X, W, 0); F_RD(XN, WN, ksn, 0, 3); F_MMA(X, W, 1); F_RD(XN, WN, ksn, 3, 3); \
      F_MMA(X, W, 2); F_RD(XN, WN, ksn, 6, 3); F_MMA(X, W, 3); F_RD(XN, WN, ksn, 9, 3); F_MMA(X, W, 4); F_RD(XN, WN, ksn, 12, 3); \
      F_MMA(X, W, 5); F_RD(XN, WN, ksn, 15, 3); } while (0)
    TS(13);
    BARRIER();
    TS(22);
#pragma unroll
    for (int r = 0; r < 18; ++r) F_READ(xA, wA, 0, r);
    SCHED();
#pragma unroll 1
    for (int c = 0; c < 6; ++c) {
      F_HEAD(xA, wA, xB, wB, 1);
      F_MMA(xA, wA, 6);
      const unsigned dx = ((0x24 >> c) & 1) ? (unsigned)(-4 * (int)SL) : 2 * SL;
#pragma unroll
      for (int t = 0; t < 4; ++t) { xa[t] += dx; PINV(xa[t]); }
      SCHED();
      F_MMA(xA, wA, 7);
#pragma unroll
      for (int t = 0; t < 4; ++t) { wa[t] += dx; PINV(wa[t]); }
      SCHED();
      F_MMA(xB, wB, 0);
      BARRIER();
      F_RD(xA, wA, 0, 0, 2); F_MMA(xB, wB, 1); F_RD(xA, wA, 0, 2, 3); F_MMA(xB, wB, 2); F_RD(xA, wA, 0, 5, 3); F_MMA(xB, wB, 3);
      F_RD(xA, wA, 0, 8, 3); F_MMA(xB, wB, 4); F_RD(xA, wA, 0, 11, 3); F_MMA(xB, wB, 5); F_RD(xA, wA, 0, 14, 2); F_MMA(xB, wB, 6);
      F_RD(xA, wA, 0, 16, 2);
      F_MMA(xB, wB, 7);
    }
    F_HEAD(xA, wA, xB, wB, 1);
    F_MMA(xA, wA, 6);
    F_MMA(xA, wA, 7);
    u32x4 hf[8];
    u32x2 ph;
    unsigned pe;
    {
      F_MMA(xB, wB, 0);
      BARRIER();
      u32x4 xg = lds_ld<u32x4>(lxg);
      bf16x8 wg[4];
#pragma unroll
      for (int t = 0; t < 4; ++t) wg[t] = lds_ld<bf16x8>(wga + t * 1024);
      SCHED();
      F_MMA(xB, wB, 1); F_MMA(xB, wB, 2); F_MMA(xB, wB, 3); F_MMA(xB, wB, 4); F_MMA(xB, wB, 5);
      const float rs = __uint_as_float(xg[3]);
      xg[3] = 0u;
      float k0 = -2.3022082f * rs, k1 = -0.10294324f * rs * rs * rs, sd = __builtin_amdgcn_rcpf(rs);
      PINV(xg); PINV(k0); PINV(k1); PINV(sd);
      SCHED();
      F_MMA(xB, wB, 6); F_MMA(xB, wB, 7);
#pragma unroll
      for (int t = 0; t < 4; ++t) MMA(acc[t], wg[t], xg);
      TS(2);
      asm volatile("s_nop 8" : "+v"(acc[0]));
      SCHED();
      static_for<0, 8>([&](auto U) __attribute__((always_inline)) {
        constexpr int u = decltype(U)::value;
#define G_DMA(q) do { if constexpr ((q) >= 0 && (q) < 4) dma1c<(q) & 3, L_WQ>(vl, wss, wqa, mw); \
          else if constexpr ((q) >= 4 && (q) < 8) WQ_B((q) & 3, L_WQ + SL, 0); \
          else if constexpr ((q) >= 8 && (q) < 12) dma1<(q) & 3, 6 * SL>(v2[(q) & 3], w2s, w2own, mw); } while (0)
        if constexpr (u == 0) pe = lpeek(C_RP, cb);
        if constexpr (u == 4) { VMCNT(5); MARK_(C_A3); }
        if constexpr (u == 6) { VMCNT(2); MARK_(C_F5); }
        if constexpr (u == 7) pe = lpeek(C_F5, cb);
        G_DMA(3 * u - 1);
        float z[8];
#define A_(e) acc[u >> 1][8 * (u & 1) + (e)]
#define Z8(c) c(z[0]), c(z[1]), c(z[2]), c(z[3]), c(z[4]), c(z[5]), c(z[6]), c(z[7])
#define A8 "v"(A_(0)), "v"(A_(1)), "v"(A_(2)), "v"(A_(3)), "v"(A_(4)), "v"(A_(5)), "v"(A_(6)), "v"(A_(7))
        asm volatile("v_mul_f32 %0, %8, %8\n\tv_mul_f32 %1, %9, %9\n\tv_mul_f32 %2, %10, %10\n\tv_mul_f32 %3, %11, %11\n\t"
                     "v_mul_f32 %4, %12, %12\n\tv_mul_f32 %5, %13, %13\n\tv_mul_f32 %6, %14, %14\n\tv_mul_f32 %7, %15, %15\n\t"
                     "v_fma_f32 %0, %0, %16, %17\n\tv_fma_f32 %1, %1, %16, %17\n\tv_fma_f32 %2, %2, %16, %17\n\tv_fma_f32 %3, %3, %16, %17\n\t"
                     "v_fma_f32 %4, %4, %16, %17\n\tv_fma_f32 %5, %5, %16, %17\n\tv_fma_f32 %6, %6, %16, %17\n\tv_fma_f32 %7, %7, %16, %17\n\t"
                     "v_mul_f32 %0, %0, %8\n\tv_mul_f32 %1, %1, %9\n\tv_mul_f32 %2, %2, %10\n\tv_mul_f32 %3, %3, %11\n\t"
                     "v_mul_f32 %4, %4, %12\n\tv_mul_f32 %5, %5, %13\n\tv_mul_f32 %6, %6, %14\n\tv_mul_f32 %7, %7, %15"
                     : Z8("=&v") : A8, "v"(k1), "v"(k0));
        if constexpr (u == 0) lcheck(C_RP, cb, pe);
        G_DMA(3 * u);
        asm volatile("v_exp_f32 %0, %0\n\tv_exp_f32 %1, %1\n\tv_exp_f32 %2, %2\n\tv_exp_f32 %3, %3\n\t"
                     "v_exp_f32 %4, %4\n\tv_exp_f32 %5, %5\n\tv_exp_f32 %6, %6\n\tv_exp_f32 %7, %7\n\t"
                     "v_fma_f32 %0, %0, %8, %8\n\tv_fma_f32 %1, %1, %8, %8\n\tv_fma_f32 %2, %2, %8, %8\n\tv_fma_f32 %3, %3, %8, %8\n\t"
                     "v_fma_f32 %4, %4, %8, %8\n\tv_fma_f32 %5, %5, %8, %8\n\tv_fma_f32 %6, %6, %8, %8\n\tv_fma_f32 %7, %7, %8, %8"
                     : Z8("+v") : "v"(sd));
        G_DMA(3 * u + 1);
        asm volatile("v_rcp_f32 %0, %0\n\tv_rcp_f32 %1, %1\n\tv_rcp_f32 %2, %2\n\tv_rcp_f32 %3, %3\n\t"
                     "v_rcp_f32 %4, %4\n\tv_rcp_f32 %5, %5\n\tv_rcp_f32 %6, %6\n\tv_rcp_f32 %7, %7\n\t"
                     "v_mul_f32 %0, %0, %8\n\tv_mul_f32 %1, %1, %9\n\tv_mul_f32 %2, %2, %10\n\tv_mul_f32 %3, %3, %11\n\t"
                     "v_mul_f32 %4, %4, %12\n\tv_mul_f32 %5, %5, %13\n\tv_mul_f32 %6, %6, %14\n\tv_mul_f32 %7, %7, %15"
                     : Z8("+v") : A8);
#pragma unroll
        for (int d = 0; d < 4; ++d) hf[u][d] = pk2(z[2 * d], z[2 * d + 1]);
        st16(hf[u], wsr, (u < 4 ? hso : hso4) + (u & 3) * 1024);
        SCHED();
      });
    }
    TS(3);
    f32x16 oc[2];
    i32x4 wos;
    unsigned oso0, ofo, wh[2], wl[2], lb2, lxr, pst, pso, lxs, xmask, oov;
#define WOO(q) (woo0 + (unsigned)((224 * ((q) < 2) + 32 * ((q) & 1)) * 896))
#define HS(n) (hs0 + ((slp >> (4 * (n))) & 7u) * 8192u)
    {
      u32x4 hr[48], sw2[12], swo[14];
#define PART(k) ((k) < 16 ? 1 + (k) / 8 : (k) < 32 ? 2 + (k) / 8 : (k) < 40 ? 3 : 6)
#define H_LOAD(k) asm volatile("buffer_load_dwordx4 %0, %1, %2, %3 offen offset:%4 sc1" : "=v"(hr[k]) \
        : "v"(((k) & 4) ? vl4 : vl), "s"(wss), "s"(HS(PART(k))), "n"(((k) & 3) * 1024) : "memory")
#define S_LOAD(q) do { if constexpr ((q) < 12) asm volatile("buffer_load_dwordx4 %0, %1, %2, %3 offen offset:%4" : "=v"(sw2[(q) % 12]) \
          : "v"(v2[(q) & 3]), "s"(w2s), "s"(W2O(4 + ((q) % 12) / 4)), "n"(((q) & 3) * 1024) : "memory"); \
        else if constexpr ((q) == 12) PEEK_ISSUE(ph, fho, 0); \
        else if constexpr ((q) < 27) asm volatile("buffer_load_dwordx4 %0, %1, %2, %3 offen offset:%4" : "=v"(swo[((q) + 1) % 14]) \
          : "v"(v2[(((q) + 1) % 7) & 3]), "s"(wos), "s"(WOO(2 * ((q) >= 20) + ((((q) + 1) % 7) >> 2))), "n"(((((q) + 1) % 7) & 3) * 1024) : "memory"); } while (0)
      bf16x4 wf[4][2][2];
#define W_ADDR(k, ct) ((k) < 0 ? wh[ct] + SL + ((k) + 8) * 2048 : (k) < 16 ? wh[ct] + 2 * SL + (k) * 2048 : (k) < 32 ? wl[ct] + ((k) - 16) * 2048 : wh[ct] + ((k) - 32) * 2048)
#define W_READ(k, ct) do { const unsigned a_ = W_ADDR(k, ct); wf[(k) & 3][ct][0] = tr1(a_); wf[(k) & 3][ct][1] = tr1(a_ + 1024); } while (0)
#define S2_LOAD(q) asm volatile("buffer_load_dwordx4 %0, %1, %2, %3 offen offset:%4" : "=v"(sw2[q]) \
          : "v"(v2[(q) & 3]), "s"(w2s), "s"(W2O(4 + (q) / 4)), "n"(((q) & 3) * 1024) : "memory")

      wos = mksrd(wo, 448 * 896);
      unsigned hs0 = WS_H + (unsigned)(((4 * i + w) * 112 + 56 * jh) * 1024), woo0 = (unsigned)(56 * w * 896 + 128 * jj);
      oso0 = WS_O + (unsigned)(((4 * i + frt) * 32 + 16 * fct) * 1024);
      unsigned lw2 = mw + vl, mwo = (unsigned)(w * 7168), lwo = mwo + vl, vl4 = vl + 4096;
      unsigned fho = (lane < 7 && lane != jj) ? WS_FL + FL_H + (unsigned)((i * 16 + 8 * jh + lane) * 4 + w) * 1024 : OOB;
      ofo = lane < 8 ? WS_FL + FL_O + (unsigned)((i * 16 + 8 * fct + lane) * 4 + frt) * 1024
          : lane == 8 ? WS_FL + FL_P + (unsigned)((i * 16 + (head ^ 8)) * 4 + frt) * 1024 : OOB;
      u64 hneed = 0x7full & ~(1ull << jj);
#pragma unroll
      for (int ct = 0; ct < 2; ++ct) {
        wl[ct] = (unsigned)((4 * hh + q4) * 128 + (g & 1) * 32 + pp * 8 + (((q4 >> 1) ^ fct ^ ct) << 6)); wh[ct] = wl[ct] + 5 * SL;
        PINV(wh[ct]); PINV(wl[ct]);
      }
      PINS(wos); PINS(hs0); PINS(woo0); PINS(oso0); PINV(lw2); PINS(mwo); PINV(lwo); PINV(vl4); PINV(fho); PINV(ofo); PINS(hneed);
      SCHED();
      lcheck(C_F5, cb, pe);
      lwait(C_A0, cb);
#pragma unroll
      for (int k = -8; k < -5; ++k) { W_READ(k, 0); W_READ(k, 1); }
      SCHED();
      VMCNT(0);
      FLAG(FL_H);
      TS(4);
      static_for<-8, 0>([&](auto K) __attribute__((always_inline)) {
        constexpr int k = decltype(K)::value;
#pragma unroll
        for (int ct = 0; ct < 2; ++ct) {
          if constexpr (k == -8) MMA0(oc[ct], cat(wf[k & 3][ct][0], wf[k & 3][ct][1]), hf[k + 8]);
          else MMA(oc[ct], cat(wf[k & 3][ct][0], wf[k & 3][ct][1]), hf[k + 8]);
          if (ct == 0) { if constexpr (0 + 2 * (k + 8) < 12) S_LOAD(0 + 2 * (k + 8)); if constexpr (k == PEEK_AT) S_LOAD(12); }
          else { if constexpr (0 + 2 * (k + 8) + 1 < 12) S_LOAD(0 + 2 * (k + 8) + 1); }
          W_READ(k + 3, ct);
          SCHED();
        }
      });
      MARK_(C_F1);
      S_LOAD(13); S_LOAD(14); S_LOAD(15); S_LOAD(16); S_LOAD(17); S_LOAD(18); S_LOAD(19);
      lb2 = L_B1 + 256 + (unsigned)(64 * fct + 8 * hh);
      lxr = L_STG + (unsigned)((32 * (w & 1) + r32) * 128 + hh * 8) + (((unsigned)(4 * fct) ^ xsw) << 4);
      pst = WS_P + (unsigned)(((i * 16 + j) * 2 + (w & 1)) * 4096) + vl;
      pso = WS_P + (unsigned)(((i * 16 + j + 7 - 14 * jh) * 2 + (w & 1)) * 4096 + fct * 2048);
      lxs = L_XCH + mw + vl; xmask = 1u << (8 * (w ^ 2));
      oov = (unsigned)((128 * i + 32 * frt + r32) * 896 + 128 * jj + 64 * fct + 16 * hh);
      PINV(lb2); PINV(lxr); PINV(pst); PINS(pso); PINV(lxs); PINS(xmask); PINV(oov);
      SCHED();
      TS(5);
      asm volatile("s_waitcnt vmcnt(7)" : "+v"(ph), "+v"(sw2[0]), "+v"(sw2[1]), "+v"(sw2[2]), "+v"(sw2[3]), "+v"(sw2[4]), "+v"(sw2[5]),
                   "+v"(sw2[6]), "+v"(sw2[7]), "+v"(sw2[8]), "+v"(sw2[9]), "+v"(sw2[10]), "+v"(sw2[11])::"memory");
      if ((SEEN(ph) & hneed) != hneed) gpoll(wsr, fho, hneed, tag);
      TS(18);
#define MIN_(a, b) ((a) < (b) ? (a) : (b))
#define NL(k) MIN_(48, (k) + 12)
#define NWO(k) ((k) <= 36 ? 0 : MIN_(7, (k) - 36))
#define NP3(k) (((k) >= 10 && (k) < NL(10)) ? 4 : 0)
      H_LOAD(0); H_LOAD(1); H_LOAD(2); H_LOAD(3); H_LOAD(4); H_LOAD(5); H_LOAD(6); H_LOAD(7); H_LOAD(8); H_LOAD(9); H_LOAD(10); H_LOAD(11);
      lwait(C_F1, cb);
      {
        unsigned l6 = lw2 + 6 * SL; PINV(l6);
#pragma unroll
        for (int q = 0; q < 4; ++q) lds_st<u32x4>(l6 + q * 1024, sw2[8 + q]);
      }
      pe = lpeek(C_A5, cb);
      unsigned lwo7 = lwo + 7 * SL; PINV(lwo7);
      SCHED();
      TS(19);
      static_for<0, 48>([&](auto K) __attribute__((always_inline)) {
        constexpr int k = decltype(K)::value;
        if constexpr (k == 1) lcheck(C_A5, cb, pe);
        if constexpr (k == 2) TS(17);
        if constexpr (k == 9) MARK_(C_F4);
        if constexpr (k == 10) dma4<5 * SL>(v2, w2s, W2O(3), mw);
        if constexpr (k == 13) lcheck(C_F4, cb, pe);
        if constexpr (k == 26) MARK_(C_F6);
        if constexpr (k == 16) { TS(20); MARK_(C_F2); }
        if constexpr (k == 20) lcheck(C_F2, cb, pe);
        if constexpr (k == 29) lcheck(C_F6, cb, pe);
        if constexpr (k == 32) { TS(16); MARK_(C_F3); }
        if constexpr (k == 36) lcheck(C_F3, cb, pe);
        if constexpr (k == 0)
          asm volatile("s_waitcnt vmcnt(%8)" : "+v"(hr[0]), "+v"(swo[0]), "+v"(swo[1]), "+v"(swo[2]), "+v"(swo[3]), "+v"(swo[4]), "+v"(swo[5]), "+v"(swo[6])
                       : "n"(NL(0) - 1) : "memory");
        else asm volatile("s_waitcnt vmcnt(%1)" : "+v"(hr[k]) : "n"(NL(k) + NP3(k) + NWO(k) + (k >= 45) - k - 1) : "memory");
        SCHED();
        constexpr int nl = NL(k + 1) - NL(k);
#pragma unroll
        for (int ct = 0; ct < 2; ++ct) {
          MMA(oc[ct], cat(wf[k & 3][ct][0], wf[k & 3][ct][1]), hr[k]);
          if (ct == 0) { if constexpr (nl > 0) H_LOAD(NL(k)); } else { if constexpr (nl == 2) H_LOAD(NL(k) + 1); }
          if constexpr (k + 3 < 48) W_READ(k + 3, ct);
          SCHED();
        }
        if constexpr (k >= 1 && k < 9) lds_st<u32x4>(lw2 + ((k - 1) >> 2) * SL + ((k - 1) & 3) * 1024, sw2[k - 1]);
        if constexpr (k == 11) pe = lpeek(C_F4, cb);
        if constexpr (k == 18) pe = lpeek(C_F2, cb);
        if constexpr (k == 27) pe = lpeek(C_F6, cb);
        if constexpr (k == 34) pe = lpeek(C_F3, cb);
        if constexpr (k >= 20 && k < 27) lds_st<u32x4>(lwo7 + (k - 20) * 1024, swo[k - 20]);
        if constexpr (k >= 36 && k < 43) dma1<(k - 36) & 3, ((k - 36) >> 2) * 4096>(v2[(k - 36) & 3], wos, WOO(2 + ((k - 36) >> 2)), mwo);
        if constexpr (k == 44) PEEK_ISSUE(ph, ofo, 0);
        SCHED();
      });
    }
    TS(6);
    u32x2 b2v[4], xres[4];
#pragma unroll
    for (int m = 0; m < 4; ++m) { b2v[m] = lds_ld<u32x2>(lb2 + 16 * m); xres[m] = lds_ld<u32x2>(lxr ^ (unsigned)(m << 4)); }
    asm volatile("s_nop 7" : "+v"(oc[0]), "+v"(oc[1]));
    if (!keeper) {
#pragma unroll
      for (int ct = 0; ct < 2; ++ct)
#pragma unroll
        for (int m = 0; m < 2; ++m)
          st16(u32x4{pk2(oc[ct][8 * m], oc[ct][8 * m + 1]), pk2(oc[ct][8 * m + 2], oc[ct][8 * m + 3]),
                     pk2(oc[ct][8 * m + 4], oc[ct][8 * m + 5]), pk2(oc[ct][8 * m + 6], oc[ct][8 * m + 7])}, wsr, pst + (1 - ct) * 2048 + m * 1024);
      oc[0] = zero16; oc[1] = zero16;
      VMCNT(4);
    } else {
      VMCNT(0);
    }
    asm volatile("" : "+v"(ph)::"memory");
    MARK_(C_WO);
    TS(7);
    u32x4 pv[2];
    bool havep;
    {
      const bool ook = (SEEN(ph) & 0xffull) == 0xffull;
      if (!ook) {
        VMCNT(0);
        if (!keeper) FLAG(FL_P);
        gpoll(wsr, ofo, 0xffull, tag);
      }
      TS(8);
      u32x4 of[16];
#define O_LOAD(k) asm volatile("buffer_load_dwordx4 %0, %1, %2, %3 offen offset:%4 sc1" : "=v"(of[k]) \
        : "v"(vl), "s"(wss), "s"(oso0 + ((k) >> 2) * 4096), "n"(((k) & 3) * 1024) : "memory")
      O_LOAD(0); O_LOAD(1); O_LOAD(2); O_LOAD(3); O_LOAD(4); O_LOAD(5); O_LOAD(6); O_LOAD(7);
      pe = lpeek(C_WO, cb);
#pragma unroll
      for (int m = 0; m < 4; ++m) {
        oc[0][4 * m] += bflo(xres[m][0]) + bflo(b2v[m][0]); oc[0][4 * m + 1] += bfhi(xres[m][0]) + bfhi(b2v[m][0]);
        oc[0][4 * m + 2] += bflo(xres[m][1]) + bflo(b2v[m][1]); oc[0][4 * m + 3] += bfhi(xres[m][1]) + bfhi(b2v[m][1]);
      }
      unsigned wb[2], pfl = (ook && !keeper) ? vfl + FL_P : OOB;
#pragma unroll
      for (int ct = 0; ct < 2; ++ct) { wb[ct] = wl[ct] + (unsigned)(fct ? 7 : 0) * SL; PINV(wb[ct]); }
      PINV(oc[0]); PINV(oc[1]); PINV(pfl);
      lcheck(C_WO, cb, pe);
      bf16x4 wf[6][2][2];
#define O_READ(k, ct) do { const unsigned a_ = wb[ct] + ((k) >> 1) * 28 * 128 + ((k) & 1) * 2048; \
        wf[(k) % 6][ct][0] = tr1(a_); wf[(k) % 6][ct][1] = tr1(a_ + 1024); } while (0)
#pragma unroll
      for (int k = 0; k < 5; ++k) { O_READ(k, 1); O_READ(k, 0); }
      SCHED();
      VMCNT(8);
      st8(tagv, wsr, pfl);
      O_LOAD(8); O_LOAD(9); O_LOAD(10); O_LOAD(11); O_LOAD(12); O_LOAD(13); O_LOAD(14); O_LOAD(15);
      TS(9);
      static_for<0, 16>([&](auto K) __attribute__((always_inline)) {
        constexpr int k = decltype(K)::value;
        if constexpr (k == P_CHECK) {
          PEEK_WAIT(ph, 0);
          havep = (SEEN(ph) >> 8) & 1;
          const unsigned pvo = havep ? vl : OOB;
          asm volatile("buffer_load_dwordx4 %0, %2, %3, %4 offen sc1\n\tbuffer_load_dwordx4 %1, %2, %3, %4 offen offset:1024 sc1"
                       : "=&v"(pv[0]), "=&v"(pv[1]) : "v"(pvo), "s"(wss), "s"(pso) : "memory");
        }
        if constexpr (k < P_CHECK) asm volatile("s_waitcnt vmcnt(%1)" : "+v"(of[k]) : "n"(15 - k + (k < 8) + (k > P_PEEK)) : "memory");
        else asm volatile("" : "+v"(of[k]));
#pragma unroll
        for (int ct = 1; ct >= 0; --ct) {
          MMA(oc[ct], cat(wf[k % 6][ct][0], wf[k % 6][ct][1]), of[k]);
          if constexpr (k + 5 < 16) O_READ(k + 5, ct);
          SCHED();
        }
        if constexpr (k == P_PEEK) PEEK_ISSUE(ph, ofo, 0);
        if constexpr (k == 12) pe = lpeek(C_A8, cb);
        SCHED();
      });
    }
    TS(10);
    lcheck(C_A8, cb, pe);
    asm volatile("s_nop 7" : "+v"(oc[1]));
#pragma unroll
    for (int q = 0; q < 4; ++q)
      lds_st<f32x4>(lxs + q * 1024, f32x4{oc[1][4 * q], oc[1][4 * q + 1], oc[1][4 * q + 2], oc[1][4 * q + 3]});
    MARK_(C_X);
    asm volatile("s_waitcnt vmcnt(0)" : "+v"(pv[0]), "+v"(pv[1])::"memory");
    if (!havep) {
      gpoll(wsr, ofo, 0x100ull, tag);
      pv[0] = ld16(wsr, vl, pso); pv[1] = ld16(wsr, vl + 1024, pso);
    }
    TS(11);
    lwait(C_X, cb, xmask);
    u32x2 ov[4];
#pragma unroll
    for (int q = 0; q < 4; ++q) {
      const unsigned p0 = pv[q >> 1][2 * (q & 1)], p1 = pv[q >> 1][2 * (q & 1) + 1];
      const f32x4 r = f32x4{oc[0][4 * q], oc[0][4 * q + 1], oc[0][4 * q + 2], oc[0][4 * q + 3]} + lds_ld<f32x4>((lxs ^ 8192u) + q * 1024) +
                      f32x4{bflo(p0), bfhi(p0), bflo(p1), bfhi(p1)};
      ov[q] = u32x2{pk2(r[0], r[1]), pk2(r[2], r[3])};
    }
#pragma unroll
    for (int q = 0; q < 4; q += 2) {
      auto s0 = __builtin_amdgcn_permlane32_swap(ov[q][0], ov[q + 1][0], false, false);
      auto s1 = __builtin_amdgcn_permlane32_swap(ov[q][1], ov[q + 1][1], false, false);
      __builtin_amdgcn_raw_buffer_store_b128(u32x4{s0[0], s1[0], s0[1], s1[1]}, __builtin_amdgcn_make_buffer_rsrc(out, 0, 2048 * 896, 0x00020000), oov + 16 * q, 0, 2);
    }
    TS(12);
    }
  }
}

extern "C" void kernel_launch(void* const* d_in, const int* in_sizes, int n_in, void* d_out, int out_size, void* d_ws,
                              size_t ws_size, hipStream_t stream) {
  fused_block<<<256, 512, 0, stream>>>((const char*)d_in[0], (const char*)d_in[1], (const char*)d_in[2], (const char*)d_in[4],
                                       (const char*)d_in[5], (const char*)d_in[6], (const char*)d_in[7], (char*)d_out,
                                       (char*)d_ws);
}
```
